```python
import torch

def _softmax(x, axis=-1):
    return torch.softmax(x, dim=axis)

def _gelu(x):
    return torch.nn.functional.gelu(x, approximate='tanh')

def _silu(x):
    return torch.nn.functional.silu(x)

def _sigmoid(x):
    return torch.sigmoid(x)

def _layernorm(x, axis=-1, eps=1e-05):
    m = x.mean(dim=axis, keepdim=True)
    v = x.var(dim=axis, keepdim=True, unbiased=False)
    return (x - m) / torch.sqrt(v + eps)

def _scatter_add(tgt, idx, src, axis=0):
    return tgt.clone().index_add_(axis, idx, src)

def _segment_sum(src, seg, n):
    out = torch.zeros((n,) + src.shape[1:], dtype=src.dtype)
    return out.index_add_(0, seg, src)

def _pad(x, pw):
    flat = tuple((v for pair in reversed(pw) for v in pair))
    return torch.nn.functional.pad(x, flat)
kernel_name = 'adaLN_cond__ssm_conv_s4__moe_arch_mod__prompt'

def setup_inputs():

    _w = lambda a: (lambda t: t.to(torch.bfloat16) if t.is_floating_point() else t)(torch.as_tensor(a))

    def _A_setup():
        B, L, D, N = (1, 2048, 448, 256)
        A_re = -(lambda _x: torch.exp(torch.as_tensor(_x)))((lambda *s: torch.randn(*s, dtype=torch.bfloat16) * 12.8)(D, N))
        A_im = torch.pi * (lambda *s: torch.randn(*s, dtype=torch.bfloat16) * 0.05)(D, N)
        B_re = (lambda *s: torch.randn(*s, dtype=torch.bfloat16) * 0.1)(D, N)
        B_im = (lambda *s: torch.randn(*s, dtype=torch.bfloat16) * 0.1)(D, N)
        C_re = (lambda *s: torch.randn(*s, dtype=torch.bfloat16) * 0.1)(D, N)
        C_im = (lambda *s: torch.randn(*s, dtype=torch.bfloat16) * 0.1)(D, N)
        log_dt = (lambda *s: torch.randn(*s, dtype=torch.bfloat16) * 0.05)(D) * 0.5 - 4.0
        Dskip = (lambda *s: torch.randn(*s, dtype=torch.bfloat16) * 0.05)(D)
        return {'u': _w((lambda *s: torch.randn(*s, dtype=torch.bfloat16) * 0.05)(B, L, D)), 'A_re': _w(A_re), 'A_im': _w(A_im), 'B_re': _w(B_re), 'B_im': _w(B_im), 'C_re': _w(C_re), 'C_im': _w(C_im), 'log_dt': _w(log_dt), 'Dskip': _w(Dskip)}
    _A_in = _A_setup()

    def _B_setup():
        B, S, D, F, C = (1, 2048, 448, 1792, 8)
        return {'x': _w((lambda *s: torch.randn(*s, dtype=torch.bfloat16) * 0.05)(B, S, D)), 'w_r': _w((lambda *s: torch.randn(*s, dtype=torch.bfloat16) * 0.05)(D)), 'w_gate': _w((lambda *s: torch.randn(*s, dtype=torch.bfloat16) * 0.05)(D, F)), 'w_up': _w((lambda *s: torch.randn(*s, dtype=torch.bfloat16) * 0.05)(D, F)), 'w_down': _w((lambda *s: torch.randn(*s, dtype=torch.bfloat16) * 0.05)(F, D))}
    _B_in = _B_setup()
    return {'A_u': _A_in['u'], 'A_A_re': _A_in['A_re'], 'A_A_im': _A_in['A_im'], 'A_B_re': _A_in['B_re'], 'A_B_im': _A_in['B_im'], 'A_C_re': _A_in['C_re'], 'A_C_im': _A_in['C_im'], 'A_log_dt': _A_in['log_dt'], 'A_Dskip': _A_in['Dskip'], 'B_x': _B_in['x'], 'B_w_r': _B_in['w_r'], 'B_w_gate': _B_in['w_gate'], 'B_w_up': _B_in['w_up'], 'B_w_down': _B_in['w_down'], 'c': _w((lambda *s: torch.randn(*s, dtype=torch.bfloat16) * 0.1)(1, 448) * 8.0), 'w_mod': _w((lambda *s: torch.randn(*s, dtype=torch.bfloat16) * 0.1)(448, 6 * 448) * 448 ** (-0.5) * 8.0)}

def reference(A_u, A_A_re, A_A_im, A_B_re, A_B_im, A_C_re, A_C_im, A_log_dt, A_Dskip, B_x, B_w_r, B_w_gate, B_w_up, B_w_down, c, w_mod):

    def _A_ref(u, A_re, A_im, B_re, B_im, C_re, C_im, log_dt, Dskip):
        Bsz, L, D = u.shape
        dt = torch.exp(log_dt)[:, None]
        A = A_re + 1j * A_im
        Bc = B_re + 1j * B_im
        Cc = C_re + 1j * C_im
        logdA = dt * A
        dB = dt * Bc
        l = (lambda x, dt: x.to(dt))(torch.arange(L), A_re.dtype)
        P = torch.exp(logdA[:, :, None] * l[None, None, :])
        K = torch.einsum('dn,dnl,dn->dl', Cc, P, dB).real
        L2 = 2 * L
        Kp = _pad(K, ((0, 0), (0, L)))
        up = (lambda x, *d: x.permute(*d))(u, 0, 2, 1)
        up = _pad(up, ((0, 0), (0, 0), (0, L)))
        Kf = (lambda x, axes=(-2, -1): torch.fft.rfft2(x.float(), dim=axes))(Kp[:, None, :], axes=(-2, -1))
        Uf = (lambda x, axes=(-2, -1): torch.fft.rfft2(x.float(), dim=axes))(up[:, :, None, :], axes=(-2, -1))
        Yf = Uf * Kf[None, :, :, :]
        y = (lambda x, s, axes=(-2, -1): torch.fft.irfft2(x, s=s, dim=axes).to(torch.bfloat16))(Yf, (1, L2), axes=(-2, -1))[:, :, 0, :L]
        y = (lambda x, *d: x.permute(*d))(y, 0, 2, 1) + u * Dskip[None, None, :]
        return y

    def _B_ref(x, w_r, w_gate, w_up, w_down):
        B, S, D = x.shape
        C = 8
        r = x @ w_r
        idx = torch.argsort(-r, axis=-1)[:, :C]
        idx_e = (lambda x, n, axis: torch.repeat_interleave(x, n, dim=axis))(idx.reshape(B, C, 1), D, axis=2)
        x_sel = (lambda x, i, axis: torch.take_along_dim(x, i, dim=axis))(x, idx_e, axis=1)
        g = _silu(x_sel @ w_gate)
        u = x_sel @ w_up
        y = g * u @ w_down
        r_sel = _sigmoid((lambda x, i, axis: torch.take_along_dim(x, i, dim=axis))(r, idx, axis=1))
        y = y * r_sel.reshape(B, C, 1)
        b_off = (torch.arange(B) * S).reshape(B, 1)
        flat_idx = (idx + b_off).reshape(B * C)
        zero = (x * 0.0).reshape(B * S, D)
        upd = _scatter_add(zero, flat_idx, y.reshape(B * C, D), axis=0)
        return x + upd.reshape(B, S, D)
    x = A_u
    mod = (_silu(c) @ w_mod).reshape(c.shape[0], 6, 1, -1)
    sa, ca, ga, sb, cb, gb = (mod[:, 0].clone(), mod[:, 1].clone(), mod[:, 2].clone(), mod[:, 3].clone(), mod[:, 4].clone(), mod[:, 5].clone())
    h = _layernorm(x, axis=-1) * (1 + ca) + sa
    x = x + ga * _A_ref(h, A_A_re, A_A_im, A_B_re, A_B_im, A_C_re, A_C_im, A_log_dt, A_Dskip)
    h = _layernorm(x, axis=-1) * (1 + cb) + sb
    x = x + gb * _B_ref(h, B_w_r, B_w_gate, B_w_up, B_w_down)
    return x
```

```cpp
#include <hip/hip_runtime.h>
#include <hip/hip_bf16.h>
#include <stdint.h>

typedef _Float16 half8 __attribute__((ext_vector_type(8)));
typedef _Float16 half4 __attribute__((ext_vector_type(4)));
typedef _Float16 half2v __attribute__((ext_vector_type(2)));
typedef __bf16 bf16x8 __attribute__((ext_vector_type(8)));
typedef __bf16 bf16x2 __attribute__((ext_vector_type(2)));
typedef float f32x4 __attribute__((ext_vector_type(4)));
typedef unsigned u32x4 __attribute__((ext_vector_type(4)));
typedef unsigned u32x2 __attribute__((ext_vector_type(2)));
typedef unsigned short u16;
#define GAS __attribute__((address_space(1)))
#define DEV __device__ __forceinline__

constexpr int L_ = 2048, D_ = 448, N_ = 256, F_ = 1792;

constexpr size_t WS_XN   = 0;
constexpr size_t WS_W    = WS_XN + (size_t)L_ * D_ * 2;
constexpr size_t WS_KIMG = WS_W + (size_t)L_ * D_ * 2;
constexpr size_t WS_KSC  = WS_KIMG + (size_t)D_ * 2048;
constexpr size_t WS_MOD  = WS_KSC + 2048;
constexpr size_t WS_R    = WS_MOD + 5632;
constexpr size_t WS_A    = WS_R + 4096;

DEV float bf2f(u16 v) { return __uint_as_float((unsigned)v << 16); }
DEV float bflo(unsigned v) { return __uint_as_float(v << 16); }
DEV float bfhi(unsigned v) { return __uint_as_float(v & 0xffff0000u); }
DEV float bfr(float x) { return (float)(__bf16)x; }
DEV unsigned pk_bf16(float a, float b) {
    bf16x2 v = {(__bf16)a, (__bf16)b};
    return __builtin_bit_cast(unsigned, v);
}
DEV unsigned pk_f16(float a, float b) {
    half2v v = {(_Float16)a, (_Float16)b};
    return __builtin_bit_cast(unsigned, v);
}
template <int CTRL> DEV float dpp_f(float v) {
    return __int_as_float(__builtin_amdgcn_update_dpp(0, __float_as_int(v), CTRL, 0xf, 0xf, true));
}
template <int CTRL> DEV unsigned dpp_u(unsigned v) {
    return (unsigned)__builtin_amdgcn_update_dpp(0, (int)v, CTRL, 0xf, 0xf, true);
}
DEV float wave_sum(float v) {
    v += dpp_f<0xB1>(v);
    v += dpp_f<0x4E>(v);
    v += dpp_f<0x141>(v);
    v += dpp_f<0x140>(v);
    {
        auto r = __builtin_amdgcn_permlane16_swap(__float_as_uint(v), __float_as_uint(v), false, false);
        v = __uint_as_float(r[0]) + __uint_as_float(r[1]);
    }
    {
        auto r = __builtin_amdgcn_permlane32_swap(__float_as_uint(v), __float_as_uint(v), false, false);
        v = __uint_as_float(r[0]) + __uint_as_float(r[1]);
    }
    return v;
}
DEV float wave_max(float v) {
    v = fmaxf(v, dpp_f<0xB1>(v));
    v = fmaxf(v, dpp_f<0x4E>(v));
    v = fmaxf(v, dpp_f<0x141>(v));
    v = fmaxf(v, dpp_f<0x140>(v));
    {
        auto r = __builtin_amdgcn_permlane16_swap(__float_as_uint(v), __float_as_uint(v), false, false);
        v = fmaxf(__uint_as_float(r[0]), __uint_as_float(r[1]));
    }
    {
        auto r = __builtin_amdgcn_permlane32_swap(__float_as_uint(v), __float_as_uint(v), false, false);
        v = fmaxf(__uint_as_float(r[0]), __uint_as_float(r[1]));
    }
    return v;
}
DEV unsigned umax_(unsigned a, unsigned b) { return a > b ? a : b; }
DEV unsigned wave_umax(unsigned v) {
    v = umax_(v, dpp_u<0xB1>(v));
    v = umax_(v, dpp_u<0x4E>(v));
    v = umax_(v, dpp_u<0x141>(v));
    v = umax_(v, dpp_u<0x140>(v));
    {
        auto r = __builtin_amdgcn_permlane16_swap(v, v, false, false);
        v = umax_(r[0], r[1]);
    }
    {
        auto r = __builtin_amdgcn_permlane32_swap(v, v, false, false);
        v = umax_(r[0], r[1]);
    }
    return v;
}
DEV void wave_fence() {
    __builtin_amdgcn_fence(__ATOMIC_RELEASE, "wavefront");
    __builtin_amdgcn_wave_barrier();
    __builtin_amdgcn_fence(__ATOMIC_ACQUIRE, "wavefront");
}
DEV int exp_above(float m) {
    int e = (int)((__float_as_uint(m) >> 23) & 0xff) - 126;
    return e < -100 ? -100 : (e > 100 ? 100 : e);
}
DEV float pow2i(int e) { return __uint_as_float((unsigned)(e + 127) << 23); }

constexpr int IMG_R1 = 0, IMG_RX = 560, IMG_RC = 608, IMG_RD = 864;
constexpr int lq_of(int l) {
    if (l < 256) return l;
    int s = l < 512 ? 1 : (l < 1024 ? 2 : 3);
    int q = l >> s, rem = l & ((1 << s) - 1), half = 1 << (s - 1);
    if (rem > half || (rem == half && (q & 1))) q++;
    return q << s;
}
constexpr int slot_of(int c) {
    if (c < 256) return (c & 15) * 16 + (c >> 4);
    if (c < 512) return (16 + ((c & 15) >> 1)) * 16 + ((c - 256) >> 4);
    if (c < 1024) return (24 + 4 * ((c - 512) >> 8) + ((c & 15) >> 2)) * 16 + ((c & 255) >> 4);
    if (c < 2048) return (32 + 2 * ((c - 1024) >> 8) + ((c & 15) >> 3)) * 16 + ((c & 255) >> 4);
    return 40 * 16;
}
struct KTab { u16 v[1024]; };
constexpr KTab make_ktab() {
    KTab t{};
    for (int n = 0; n < 1024; ++n) t.v[n] = 768;
    for (int n = 0; n < 560; ++n) { int l = 543 - n; if (l >= 0 && l <= 514) t.v[IMG_R1 + n] = (u16)slot_of(lq_of(l)); }
    for (int n = 0; n < 48; ++n) { int l = 1055 - n; if (l == 1027 || l == 1028) t.v[IMG_RX + n] = (u16)slot_of(1024); }
    const int coffC[4] = {16, 4, 8, 12};
    for (int e = 0; e < 4; ++e)
        for (int n = 0; n < 64; ++n) { int lam = 79 - n; if (lam >= 32 && lam <= 63) t.v[IMG_RC + e * 64 + n] = (u16)slot_of(16 * lam + coffC[e]); }
    const int coffD[2] = {16, 8};
    for (int e = 0; e < 2; ++e)
        for (int n = 0; n < 80; ++n) { int lam = 127 - n; if (lam >= 64 && lam <= 127) t.v[IMG_RD + e * 80 + n] = (u16)slot_of(16 * lam + coffD[e]); }
    return t;
}
__device__ const KTab g_ktab = make_ktab();

struct cf { float r, i; };
DEV cf cmul(cf a, cf b) { return {a.r * b.r - a.i * b.i, a.r * b.i + a.i * b.r}; }
DEV cf csqr(cf a) { return {a.r * a.r - a.i * a.i, (a.r + a.r) * a.i}; }
DEV cf cexp_pow2(float t2, float rh, float rl, float Lf) {
    float mag = __builtin_amdgcn_exp2f(t2 * Lf);
    float a = rh * Lf;
    float ph = (a - __builtin_rintf(a)) + rl * Lf;
    return {mag * __builtin_amdgcn_cosf(ph), mag * __builtin_amdgcn_sinf(ph)};
}

constexpr int NB_KG = 448, NB_MOD = 84, NB_LN = 256;
constexpr int KG_ROW = 288;
constexpr int KG_BUF = 16 * KG_ROW;
constexpr int KG_WAVE = 2 * KG_BUF;
constexpr int SM0 = 4 * KG_WAVE + 1600 + 64;

__global__ __launch_bounds__(256) void k0_prep(
    const u16* __restrict__ x, const u16* __restrict__ A_re, const u16* __restrict__ A_im,
    const u16* __restrict__ B_re, const u16* __restrict__ B_im, const u16* __restrict__ C_re,
    const u16* __restrict__ C_im, const u16* __restrict__ log_dt, const u16* __restrict__ cvec,
    const u16* __restrict__ w_mod, unsigned char* __restrict__ ws)
{
    __shared__ __attribute__((aligned(16))) unsigned char smem[SM0];
    const int tid = threadIdx.x, lane = tid & 63, w = tid >> 6;
    const int b = blockIdx.x;
    if (b < NB_KG) {
        const int d = b, n = tid;
        const u32x2 kidx = *(const u32x2*)(&g_ktab.v[4 * tid]);
        const float are = bf2f(A_re[d * N_ + n]), aim = bf2f(A_im[d * N_ + n]);
        const float bre = bf2f(B_re[d * N_ + n]), bim = bf2f(B_im[d * N_ + n]);
        const float cre = bf2f(C_re[d * N_ + n]), cim = bf2f(C_im[d * N_ + n]);
        const float dt = bfr(expf(bf2f(log_dt[d])));
        float zr = dt * are;
        const float zi = dt * aim;
        const float dbr = dt * bre, dbi = dt * bim;
        cf cb = {cre * dbr - cim * dbi, cre * dbi + cim * dbr};
        const float mx = wave_max(fmaxf(fabsf(cb.r), fabsf(cb.i)));
        const float rho = fmaxf(wave_max(zr), 0.f);
        zr -= rho;
        const int ew = exp_above(mx);
        const float sc = pow2i(14 - ew < 120 ? 14 - ew : 120);
        cb.r *= sc; cb.i *= sc;
        const float t2 = zr * 1.44269504088896341f;
        const float rh = zi * 0.15915494309189535f;
        const float rl = __builtin_fmaf(zi, 0.15915494309189535f, -rh) + zi * 6.4206382e-09f;
        unsigned char* wb = smem + w * KG_WAVE;
        unsigned* st0 = (unsigned*)(wb + lane * 4);
        unsigned* st1 = (unsigned*)(wb + KG_BUF + lane * 4);
        const int fr = lane & 15, fq = lane >> 4;
        const unsigned char* rd0 = wb + fr * KG_ROW + fq * 16;
        const unsigned char* rd1 = rd0 + KG_BUF;
#define ST(buf, row, v) buf[(row) * (KG_ROW / 4)] = pk_f16((v).r, (v).i)
#define STC(buf, row, v) buf[(row) * (KG_ROW / 4)] = pk_f16((v).r, -(v).i)
        {
            cf s1 = cexp_pow2(t2, rh, rl, 16.f), s2 = csqr(s1), s4 = csqr(s2), s8 = csqr(s4);
            cf q0 = cb, q1 = cmul(q0, s1), q2 = cmul(q0, s2), q3 = cmul(q1, s2);
            cf q4 = cmul(q0, s4), q5 = cmul(q1, s4), q6 = cmul(q2, s4), q7 = cmul(q3, s4);
            STC(st0, 0, q0); STC(st0, 1, q1); STC(st0, 2, q2); STC(st0, 3, q3);
            STC(st0, 4, q4); STC(st0, 5, q5); STC(st0, 6, q6); STC(st0, 7, q7);
            cf t;
            t = cmul(q0, s8); STC(st0, 8, t);  t = cmul(q1, s8); STC(st0, 9, t);
            t = cmul(q2, s8); STC(st0, 10, t); t = cmul(q3, s8); STC(st0, 11, t);
            t = cmul(q4, s8); STC(st0, 12, t); t = cmul(q5, s8); STC(st0, 13, t);
            t = cmul(q6, s8); STC(st0, 14, t); t = cmul(q7, s8); STC(st0, 15, t);
        }
        cf p1 = cexp_pow2(t2, rh, rl, 1.f), p2 = csqr(p1), p3 = cmul(p2, p1), p4 = csqr(p2);
        cf p5 = cmul(p4, p1), p6 = cmul(p4, p2), p7 = cmul(p4, p3), p8 = csqr(p4);
        cf p10 = cmul(p8, p2), p12 = cmul(p8, p4), p14 = cmul(p8, p6);
        {
            cf one = {1.f, 0.f}, t;
            ST(st1, 0, one); ST(st1, 1, p1); ST(st1, 2, p2); ST(st1, 3, p3);
            ST(st1, 4, p4); ST(st1, 5, p5); ST(st1, 6, p6); ST(st1, 7, p7);
            ST(st1, 8, p8); t = cmul(p8, p1); ST(st1, 9, t); ST(st1, 10, p10); t = cmul(p8, p3); ST(st1, 11, t);
            ST(st1, 12, p12); t = cmul(p8, p5); ST(st1, 13, t); ST(st1, 14, p14); t = cmul(p8, p7); ST(st1, 15, t);
        }
        wave_fence();
        half8 xf[4];
        f32x4 acc0 = {0, 0, 0, 0}, acc1 = {0, 0, 0, 0}, acc2 = {0, 0, 0, 0};
#pragma unroll
        for (int s = 0; s < 4; ++s) xf[s] = *(const half8*)(rd0 + s * 64);
#pragma unroll
        for (int s = 0; s < 4; ++s) {
            half8 f = *(const half8*)(rd1 + s * 64);
            acc0 = __builtin_amdgcn_mfma_f32_16x16x32_f16(xf[s], f, acc0, 0, 0, 0);
        }
        wave_fence();
        {
            cf g1 = cexp_pow2(t2, rh, rl, 256.f), g2 = csqr(g1), g3 = cmul(g2, g1), g4 = csqr(g2);
            cf t;
            ST(st0, 0, g1); t = cmul(g1, p2); ST(st0, 1, t); t = cmul(g1, p4); ST(st0, 2, t); t = cmul(g1, p6); ST(st0, 3, t);
            t = cmul(g1, p8); ST(st0, 4, t); t = cmul(g1, p10); ST(st0, 5, t); t = cmul(g1, p12); ST(st0, 6, t); t = cmul(g1, p14); ST(st0, 7, t);
            ST(st0, 8, g2); t = cmul(g2, p4); ST(st0, 9, t); t = cmul(g2, p8); ST(st0, 10, t); t = cmul(g2, p12); ST(st0, 11, t);
            ST(st0, 12, g3); t = cmul(g3, p4); ST(st0, 13, t); t = cmul(g3, p8); ST(st0, 14, t); t = cmul(g3, p12); ST(st0, 15, t);
            cf g5 = cmul(g4, g1), g6 = cmul(g4, g2), g7 = cmul(g4, g3), g8 = csqr(g4);
            ST(st1, 0, g4); t = cmul(g4, p8); ST(st1, 1, t);
            ST(st1, 2, g5); t = cmul(g5, p8); ST(st1, 3, t);
            ST(st1, 4, g6); t = cmul(g6, p8); ST(st1, 5, t);
            ST(st1, 6, g7); t = cmul(g7, p8); ST(st1, 7, t);
            ST(st1, 8, g8);
        }
        wave_fence();
#pragma unroll
        for (int s = 0; s < 4; ++s) {
            half8 f = *(const half8*)(rd0 + s * 64);
            acc1 = __builtin_amdgcn_mfma_f32_16x16x32_f16(xf[s], f, acc1, 0, 0, 0);
            half8 g = *(const half8*)(rd1 + s * 64);
            acc2 = __builtin_amdgcn_mfma_f32_16x16x32_f16(xf[s], g, acc2, 0, 0, 0);
        }
        wave_fence();
#undef ST
#undef STC
        float* P = (float*)wb;
        *(f32x4*)(P + (0 + fr) * 20 + 4 * fq) = acc0;
        *(f32x4*)(P + (16 + fr) * 20 + 4 * fq) = acc1;
        *(f32x4*)(P + (32 + fr) * 20 + 4 * fq) = acc2;
        float* meta = (float*)(smem + 4 * KG_WAVE + 1600);
        if (lane == 0) { meta[w] = (float)ew; meta[4 + w] = rho; }
        __syncthreads();
        u16* T16 = (u16*)(smem + 4 * KG_WAVE);
        const float e0 = meta[0], e1 = meta[1], e2 = meta[2], e3 = meta[3];
        const float r0 = meta[4], r1 = meta[5], r2 = meta[6], r3 = meta[7];
        const float rmax = fmaxf(fmaxf(r0, r1), fmaxf(r2, r3));
        int eM = (int)fmaxf(fmaxf(e0, e1), fmaxf(e2, e3));
        if (rmax > 0.f) {
            float ex = ceilf(rmax * (2048.f * 1.44269504088896341f));
            eM += (int)fminf(ex, 100.f);
        }
        eM = eM > 110 ? 110 : eM;
        if (tid < 192) {
            const int row = tid >> 2, c4 = tid & 3;
            f32x4 s = {0, 0, 0, 0};
#pragma unroll
            for (int ww = 0; ww < 4; ++ww) {
                const float* Pw = (const float*)(smem + ww * KG_WAVE);
                f32x4 v = *(const f32x4*)(Pw + row * 20 + 4 * c4);
                const int ewv = (int)meta[ww];
                const int sh0 = 14 - ewv < 120 ? 14 - ewv : 120;
                int sh = (6 - eM) - sh0;
                float f1 = pow2i(sh < -120 ? -120 : sh), f2 = sh < -120 ? pow2i(sh + 120 < -120 ? -120 : sh + 120) : 1.f;
                if (rmax > 0.f) {
                    const int hi = row < 16 ? row : (row < 24 ? 256 + 2 * (row - 16) : (row < 32 ? 512 + 256 * ((row - 24) >> 2) + 4 * (row & 3)
                                 : (row < 40 ? 1024 + 256 * ((row - 32) >> 1) + 8 * (row & 1) : 2048)));
                    const float rw = meta[4 + ww] * 1.44269504088896341f;
#pragma unroll
                    for (int j = 0; j < 4; ++j) v[j] = v[j] * f1 * exp2f(rw * (float)(hi + 16 * (4 * c4 + j))) * f2;
                } else {
#pragma unroll
                    for (int j = 0; j < 4; ++j) v[j] = v[j] * f1 * f2;
                }
                s += v;
            }
            u32x2 o = {pk_f16(s[0], s[1]), pk_f16(s[2], s[3])};
            *(u32x2*)(T16 + row * 16 + 4 * c4) = o;
        } else if (tid == 192) {
            T16[768] = 0;
            ((float*)(ws + WS_KSC))[d] = pow2i(eM - 6);
        }
        __syncthreads();
        u32x2 o;
        o[0] = (unsigned)T16[kidx[0] & 0xffff] | ((unsigned)T16[kidx[0] >> 16] << 16);
        o[1] = (unsigned)T16[kidx[1] & 0xffff] | ((unsigned)T16[kidx[1] >> 16] << 16);
        *(u32x2*)(ws + WS_KIMG + (size_t)d * 2048 + tid * 8) = o;
    } else if (b < NB_KG + NB_MOD) {
        const int cb0 = (b - NB_KG) * 32, rg = tid >> 2, qt = tid & 3;
        u32x4 wv[7];
        float sv[7];
#pragma unroll
        for (int k = 0; k < 7; ++k) {
            const int row = rg + 64 * k;
            wv[k] = *(const u32x4*)(w_mod + (size_t)row * (6 * D_) + cb0 + qt * 8);
            sv[k] = bf2f(cvec[row]);
        }
        float acc[8] = {0, 0, 0, 0, 0, 0, 0, 0};
#pragma unroll
        for (int k = 0; k < 7; ++k) {
            const float s = bfr(sv[k] / (1.f + expf(-sv[k])));
#pragma unroll
            for (int j = 0; j < 4; ++j) {
                acc[2 * j] = __builtin_fmaf(s, bflo(wv[k][j]), acc[2 * j]);
                acc[2 * j + 1] = __builtin_fmaf(s, bfhi(wv[k][j]), acc[2 * j + 1]);
            }
        }
        float* red = (float*)smem;
#pragma unroll
        for (int j = 0; j < 8; ++j) red[rg * 33 + qt * 8 + j] = acc[j];
        __syncthreads();
        {
            const int col = tid & 31, part = tid >> 5;
            float s = 0.f;
#pragma unroll
            for (int k = 0; k < 8; ++k) s += red[(part * 8 + k) * 33 + col];
            __syncthreads();
            red[part * 33 + col] = s;
            __syncthreads();
            if (tid < 32) {
                float t = 0.f;
#pragma unroll
                for (int k = 0; k < 8; ++k) t += red[k * 33 + tid];
                ((u16*)(ws + WS_MOD))[cb0 + tid] = (u16)(pk_bf16(t, 0.f) & 0xffff);
            }
        }
    } else {
        const int g = b - NB_KG - NB_MOD;
        const int t0 = g * 8;
        const bool act = lane < 56;
        u32x4 xv[2];
#pragma unroll
        for (int k = 0; k < 2; ++k) {
            xv[k] = (u32x4){0, 0, 0, 0};
            if (act) xv[k] = *(const u32x4*)(x + (size_t)(t0 + 2 * w + k) * D_ + lane * 8);
        }
        unsigned* tile = (unsigned*)smem;
#pragma unroll
        for (int k = 0; k < 2; ++k) {
            float f[8];
#pragma unroll
            for (int j = 0; j < 4; ++j) { f[2 * j] = bflo(xv[k][j]); f[2 * j + 1] = bfhi(xv[k][j]); }
            float s = 0.f;
#pragma unroll
            for (int j = 0; j < 8; ++j) s += f[j];
            const float mean = wave_sum(s) * (1.f / D_);
            float q = 0.f;
#pragma unroll
            for (int j = 0; j < 8; ++j) { float dd = f[j] - mean; q = __builtin_fmaf(dd, dd, q); }
            q = act ? q : 0.f;
            const float var = wave_sum(q) * (1.f / D_);
            const float mb = bfr(mean);
            const float sd = bfr(sqrtf(bfr(bfr(var) + 1e-5f)));
            u32x4 o;
#pragma unroll
            for (int j = 0; j < 4; ++j) o[j] = pk_bf16(bfr(f[2 * j] - mb) / sd, bfr(f[2 * j + 1] - mb) / sd);
            if (act) *(u32x4*)(tile + (2 * w + k) * 228 + lane * 4) = o;
        }
        __syncthreads();
#pragma unroll
        for (int it = 0; it < 2; ++it) {
            const int e = tid + 256 * it;
            if (e < 448) {
                const int pr = e >> 1, hf = e & 1;
                u32x4 o;
#pragma unroll
                for (int j = 0; j < 4; ++j) o[j] = tile[(4 * hf + j) * 228 + pr];
                *(u32x4*)(ws + WS_XN + (size_t)pr * 8192 + (t0 + 4 * hf) * 4) = o;
            }
        }
    }
}

constexpr int U_PAD = 256;
constexpr int U_LEN = U_PAD + L_;
constexpr int SM1_U = 0;
constexpr int SM1_H = 2 * U_LEN * 2;
constexpr int SM1_R = SM1_H + 2 * L_ * 2;
constexpr int SM1 = SM1_R + 16384;

DEV half8 ld_img16(const unsigned char* p) {
    typedef u32x4 __attribute__((aligned(2))) u32x4u;
    u32x4 v = *(const u32x4u*)p;
    return __builtin_bit_cast(half8, v);
}
DEV half8 ld_img8x2(const unsigned char* pa, const unsigned char* pb) {
    typedef u32x2 __attribute__((aligned(2))) u32x2u;
    u32x2 a = *(const u32x2u*)pa, b = *(const u32x2u*)pb;
    u32x4 v = {a[0], a[1], b[0], b[1]};
    return __builtin_bit_cast(half8, v);
}
DEV half8 box_frag(int p, int koff, int phif, int wd) {
    half8 r;
#pragma unroll
    for (int j = 0; j < 8; ++j) {
        const int o = koff + j, hi = p - phif;
        r[j] = (o <= hi && o >= hi - (wd - 1)) ? (_Float16)1.f : (_Float16)0.f;
    }
    return r;
}
DEV half8 pack_v(f32x4 a, f32x4 b) {
    u32x4 v = {pk_f16(a[0], a[1]), pk_f16(a[2], a[3]), pk_f16(b[0], b[1]), pk_f16(b[2], b[3])};
    return __builtin_bit_cast(half8, v);
}
#define MFMA16(a, b, c) __builtin_amdgcn_mfma_f32_16x16x32_f16(a, b, c, 0, 0, 0)

__global__ __launch_bounds__(256) void k1_conv(const u16* __restrict__ Dskip, unsigned char* __restrict__ ws)
{
    __shared__ __attribute__((aligned(16))) unsigned char smem[SM1];
    const int tid = threadIdx.x, lane = tid & 63;
    const int w = __builtin_amdgcn_readfirstlane(tid >> 6);
    const int pair = (blockIdx.x & 7) * 28 + (blockIdx.x >> 3);
    const int c = w >> 1, hs = w & 1;
    const int d = 2 * pair + c;
    const int p = lane & 15, q = lane >> 4;

    const u32x4* xn = (const u32x4*)(ws + WS_XN + (size_t)pair * 8192);
    const u32x4 xv0 = xn[tid], xv1 = xn[tid + 256];
    const u16* modp = (const u16*)(ws + WS_MOD);
    const unsigned sa2 = *(const unsigned*)(modp + 2 * pair);
    const unsigned ca2 = *(const unsigned*)(modp + D_ + 2 * pair);
    const float ga = bf2f(modp[2 * D_ + d]);
    const float dsk = bf2f(Dskip[d]);
    const float invK = ((const float*)(ws + WS_KSC))[d];
    const unsigned char* img = ws + WS_KIMG + (size_t)d * 2048;
    const int base1 = 543 - 16 * (q >> 1) - p + 8 * (q & 1);
    half8 ka[8];
#pragma unroll
    for (int j = 0; j < 4; ++j) {
        ka[j] = ld_img16(img + 2 * (IMG_R1 + base1 - 32 * (4 * hs + j)));
        ka[4 + j] = ld_img16(img + 2 * (IMG_R1 + base1 - 32 * (8 + 4 * hs + j)));
    }
    const int ea = hs ? 0 : 1, eb = hs ? 3 : 2;
    half8 kc[3];
#pragma unroll
    for (int s = 0; s < 3; ++s) {
        const int n = 79 - 16 * (s + 2) - p + 4 * q;
        kc[s] = ld_img8x2(img + 2 * (IMG_RC + ea * 64 + n), img + 2 * (IMG_RC + eb * 64 + n));
    }
    half8 kx[4];
    if (hs == 0) {
        kx[0] = ld_img16(img + 2 * (IMG_R1 + base1 - 32 * 16));
        kx[1] = ld_img16(img + 2 * (IMG_RX + (1055 - 1024) - 16 * (q >> 1) - p + 8 * (q & 1)));
        kx[2] = kx[0]; kx[3] = kx[0];
    } else {
#pragma unroll
        for (int s = 0; s < 4; ++s) {
            const int n = 127 - 16 * (s + 4) - p + 4 * q;
            kx[s] = ld_img8x2(img + 2 * (IMG_RD + n), img + 2 * (IMG_RD + 80 + n));
        }
    }

    const float opc0 = bfr(1.f + bflo(ca2)), opc1 = bfr(1.f + bfhi(ca2));
    const float sa0 = bflo(sa2), sa1 = bfhi(sa2);
    const int eu0 = exp_above(32.f * fabsf(opc0) + fabsf(sa0)), eu1 = exp_above(32.f * fabsf(opc1) + fabsf(sa1));
    const float su0 = pow2i(11 - eu0), su1 = pow2i(11 - eu1);
    u16* U0 = (u16*)(smem + SM1_U);
    u16* U1 = U0 + U_LEN;
    u16* H0 = (u16*)(smem + SM1_H);
    u16* H1 = H0 + L_;
    if (tid < 128) ((unsigned*)(tid < 64 ? U0 : U1))[(tid & 63) * 2] = 0, ((unsigned*)(tid < 64 ? U0 : U1))[(tid & 63) * 2 + 1] = 0;
#pragma unroll
    for (int k = 0; k < 2; ++k) {
        const u32x4 xv = k ? xv1 : xv0;
        const int t = 4 * (tid + 256 * k);
        float h0[4], h1[4];
#pragma unroll
        for (int j = 0; j < 4; ++j) {
            h0[j] = bfr(bfr(bflo(xv[j]) * opc0) + sa0);
            h1[j] = bfr(bfr(bfhi(xv[j]) * opc1) + sa1);
        }
        *(u32x2*)(U0 + U_PAD + t) = (u32x2){pk_f16(h0[0] * su0, h0[1] * su0), pk_f16(h0[2] * su0, h0[3] * su0)};
        *(u32x2*)(U1 + U_PAD + t) = (u32x2){pk_f16(h1[0] * su1, h1[1] * su1), pk_f16(h1[2] * su1, h1[3] * su1)};
        *(u32x2*)(H0 + t) = (u32x2){pk_bf16(h0[0], h0[1]), pk_bf16(h0[2], h0[3])};
        *(u32x2*)(H1 + t) = (u32x2){pk_bf16(h1[0], h1[1]), pk_bf16(h1[2], h1[3])};
    }
    __syncthreads();

    const int koffb = -16 * (q >> 1) + 8 * (q & 1);
    const u16* Uc = (c ? U1 : U0) + U_PAD + 16 * p + koffb;
    f32x4 acc[8];
#pragma unroll
    for (int i = 0; i < 8; ++i) acc[i] = (f32x4){0, 0, 0, 0};
    const f32x4 zero4 = {0, 0, 0, 0};

    if (hs == 0) {
        const half8 t1 = box_frag(p, koffb, 3, 3), t2 = box_frag(p, koffb, 6, 5);
#pragma unroll
        for (int m = 0; m < 8; ++m) {
            const half8 bf = *(const half8*)(Uc + 32 * (8 * m));
            acc[m] = MFMA16(ka[0], bf, acc[m]);
            if (m + 1 < 8) acc[m + 1] = MFMA16(ka[4], bf, acc[m + 1]);
            if (m + 2 < 8) acc[m + 2] = MFMA16(kx[0], bf, acc[m + 2]);
            if (m + 4 < 8) acc[m + 4] = MFMA16(kx[1], bf, acc[m + 4]);
            if (m < 6) {
                const f32x4 va = MFMA16(bf, t1, zero4), vb = MFMA16(bf, t2, zero4);
                const half8 fa = pack_v(va, vb);
#pragma unroll
                for (int s = 0; s < 3; ++s)
                    if (m + s + 2 < 8) acc[m + s + 2] = MFMA16(fa, kc[s], acc[m + s + 2]);
            }
        }
#pragma unroll
        for (int r = 1; r < 4; ++r)
#pragma unroll
            for (int m = 0; m < 8; ++m) {
                const half8 bf = *(const half8*)(Uc + 32 * (8 * m - r));
                acc[m] = MFMA16(ka[r], bf, acc[m]);
                if (m + 1 < 8) acc[m + 1] = MFMA16(ka[4 + r], bf, acc[m + 1]);
            }
    } else {
        const half8 t0 = box_frag(p, koffb - 8, 14, 5), t3 = box_frag(p, koffb - 8, 11, 3);
        const half8 d0 = box_frag(p, koffb - 8, 12, 9), d1 = box_frag(p, koffb, 5, 7);
#pragma unroll
        for (int m = 0; m < 6; ++m) {
            const half8 wa = *(const half8*)(Uc + 32 * (8 * m) - 8);
            const f32x4 va = MFMA16(wa, t0, zero4), vb = MFMA16(wa, t3, zero4);
            const half8 fa = pack_v(va, vb);
#pragma unroll
            for (int s = 0; s < 3; ++s)
                if (m + s + 2 < 8) acc[m + s + 2] = MFMA16(fa, kc[s], acc[m + s + 2]);
            if (m < 4) {
                const half8 wb = *(const half8*)(Uc + 32 * (8 * m));
                const f32x4 vc = MFMA16(wa, d0, zero4), vd = MFMA16(wb, d1, zero4);
                const half8 fd = pack_v(vc, vd);
#pragma unroll
                for (int s = 0; s < 4; ++s)
                    if (m + s + 4 < 8) acc[m + s + 4] = MFMA16(fd, kx[s], acc[m + s + 4]);
            }
        }
#pragma unroll
        for (int r = 0; r < 4; ++r)
#pragma unroll
            for (int m = 0; m < 8; ++m) {
                const half8 bf = *(const half8*)(Uc + 32 * (8 * m - 4 - r));
                acc[m] = MFMA16(ka[r], bf, acc[m]);
                if (m + 1 < 8) acc[m + 1] = MFMA16(ka[4 + r], bf, acc[m + 1]);
            }
    }

    f32x4* red = (f32x4*)(smem + SM1_R);
    f32x4 mine[4];
    if (hs == 0) {
#pragma unroll
        for (int i = 0; i < 4; ++i) { red[(w * 4 + i) * 64 + lane] = acc[4 + i]; mine[i] = acc[i]; }
    } else {
#pragma unroll
        for (int i = 0; i < 4; ++i) { red[(w * 4 + i) * 64 + lane] = acc[i]; mine[i] = acc[4 + i]; }
    }
    __syncthreads();
    const float ysc = invK * pow2i((c ? eu1 : eu0) - 11);
    const u16* Hc = c ? H1 : H0;
    u32x2 pkd[4];
#pragma unroll
    for (int i = 0; i < 4; ++i) {
        const f32x4 o = red[((w ^ 1) * 4 + i) * 64 + lane];
        const int t = 16 * (16 * (4 * hs + i) + p) + 4 * q;
        const u32x2 hv = *(const u32x2*)(Hc + t);
        float r[4];
#pragma unroll
        for (int j = 0; j < 4; ++j) {
            const float y = bfr((mine[i][j] + o[j]) * ysc);
            const float hh = (j & 1) ? bfhi(hv[j >> 1]) : bflo(hv[j >> 1]);
            const float z = bfr(y + bfr(hh * dsk));
            r[j] = ga * z;
        }
        pkd[i] = (u32x2){pk_bf16(r[0], r[1]), pk_bf16(r[2], r[3])};
    }
#pragma unroll
    for (int i = 0; i < 4; i += 2) {
        auto s0 = __builtin_amdgcn_permlane16_swap(pkd[i][0], pkd[i + 1][0], false, false);
        auto s1 = __builtin_amdgcn_permlane16_swap(pkd[i][1], pkd[i + 1][1], false, false);
        const int tile = 4 * hs + i + (q & 1);
        const int g = 2 * (16 * tile + p) + (q >> 1);
        *(u32x4*)(ws + WS_W + (size_t)g * (D_ * 16) + d * 16) = (u32x4){s0[0], s1[0], s0[1], s1[1]};
    }
}

struct Tok { float x1[8], h2[8]; };
DEV void ln2_core(const float (&x1)[8], bool act, u32x4 sbv, u32x4 cbv, float (&h2)[8]) {
    float s = 0.f;
#pragma unroll
    for (int j = 0; j < 8; ++j) s += x1[j];
    s = act ? s : 0.f;
    const float mean = wave_sum(s) * (1.f / D_);
    float q = 0.f;
#pragma unroll
    for (int j = 0; j < 8; ++j) { float dd = x1[j] - mean; q = __builtin_fmaf(dd, dd, q); }
    q = act ? q : 0.f;
    const float var = wave_sum(q) * (1.f / D_);
    const float mb = bfr(mean);
    const float sd = bfr(sqrtf(bfr(bfr(var) + 1e-5f)));
#pragma unroll
    for (int j = 0; j < 8; ++j) {
        const float cbj = (j & 1) ? bfhi(cbv[j >> 1]) : bflo(cbv[j >> 1]);
        const float sbj = (j & 1) ? bfhi(sbv[j >> 1]) : bflo(sbv[j >> 1]);
        const float xn = bfr(bfr(x1[j] - mb) / sd);
        h2[j] = bfr(bfr(xn * bfr(1.f + cbj)) + sbj);
    }
}

__global__ __launch_bounds__(512) void k2_ln2(const u16* __restrict__ x, const u16* __restrict__ w_r,
                                              unsigned char* __restrict__ ws, u16* __restrict__ out)
{
    __shared__ __attribute__((aligned(16))) unsigned tile[8 * 228];
    const int tid = threadIdx.x, lane = tid & 63, w = tid >> 6;
    const int g = blockIdx.x, t = g * 8 + w;
    const bool act = lane < 56;
    const int lo = act ? lane : 0;
    u32x4 wa = {0, 0, 0, 0}, wb = {0, 0, 0, 0};
    if (tid < 224) {
        const u32x4* wp = (const u32x4*)(ws + WS_W + (size_t)g * (D_ * 16) + tid * 32);
        wa = wp[0]; wb = wp[1];
    }
    const u32x4 xv = *(const u32x4*)(x + (size_t)t * D_ + lo * 8);
    const u16* modp = (const u16*)(ws + WS_MOD);
    const u32x4 sbv = *(const u32x4*)(modp + 3 * D_ + lo * 8);
    const u32x4 cbv = *(const u32x4*)(modp + 4 * D_ + lo * 8);
    const u32x4 gbv = *(const u32x4*)(modp + 5 * D_ + lo * 8);
    const u32x4 wrv = *(const u32x4*)(w_r + lo * 8);
    if (g < 7) {
        ((u32x2*)(ws + WS_A))[g * 512 + tid] = (u32x2){0xffffffffu, 0xffffffffu};
    }
    if (tid < 224) {
#pragma unroll
        for (int j = 0; j < 4; ++j) {
            tile[(2 * j) * 228 + tid] = (wa[j] & 0xffffu) | (wb[j] << 16);
            tile[(2 * j + 1) * 228 + tid] = (wa[j] >> 16) | (wb[j] & 0xffff0000u);
        }
    }
    __syncthreads();
    const u32x4 wv = *(const u32x4*)(tile + w * 228 + lo * 4);
    float x1[8], h2[8];
#pragma unroll
    for (int j = 0; j < 4; ++j) {
        x1[2 * j] = bfr(bflo(xv[j]) + bflo(wv[j]));
        x1[2 * j + 1] = bfr(bfhi(xv[j]) + bfhi(wv[j]));
    }
    ln2_core(x1, act, sbv, cbv, h2);
    float r = 0.f;
    u32x4 o;
#pragma unroll
    for (int j = 0; j < 4; ++j) {
        r = __builtin_fmaf(h2[2 * j], bflo(wrv[j]), r);
        r = __builtin_fmaf(h2[2 * j + 1], bfhi(wrv[j]), r);
        o[j] = pk_bf16(x1[2 * j] + bfr(bflo(gbv[j]) * h2[2 * j]), x1[2 * j + 1] + bfr(bfhi(gbv[j]) * h2[2 * j + 1]));
    }
    r = act ? r : 0.f;
    r = wave_sum(r);
    if (act) *(u32x4*)(out + (size_t)t * D_ + lane * 8) = o;
    if (lane == 0) ((u16*)(ws + WS_R))[t] = (u16)(pk_bf16(r, 0.f) & 0xffff);
}

constexpr int NB_FFN = 56;
constexpr int SM3_WG = 0;
constexpr int SM3_WU = 28672;
constexpr int SM3_WD = 57344;
constexpr int SM3_H2 = 86016;
constexpr int SM3_X1 = SM3_H2 + 8 * 912;
constexpr int SM3_P  = SM3_X1 + 512;
constexpr int SM3_K  = SM3_P + 4096;
constexpr int SM3 = SM3_K + 256;

DEV u32x2 tr_read(unsigned addr) {
    u32x2 r;
    asm volatile("ds_read_b64_tr_b16 %0, %1\n\ts_waitcnt lgkmcnt(0)" : "=&v"(r) : "v"(addr) : "memory");
    return r;
}
#define MFMAB(a, b, c) __builtin_amdgcn_mfma_f32_16x16x32_bf16(a, b, c, 0, 0, 0)

__global__ __launch_bounds__(512) void k3_ffn(const u16* __restrict__ x, const u16* __restrict__ w_gate,
                                              const u16* __restrict__ w_up, const u16* __restrict__ w_down,
                                              unsigned char* __restrict__ ws, u16* __restrict__ out)
{
    __shared__ __attribute__((aligned(16))) unsigned char smem[SM3];
    const int tid = threadIdx.x, lane = tid & 63;
    const int w = __builtin_amdgcn_readfirstlane(tid >> 6);
    const int b = (blockIdx.x & 7) * 7 + (blockIdx.x >> 3);
    const u32x2 rv = ((const u32x2*)(ws + WS_R))[tid];
    u32x4 wg[7], wd[4];
#pragma unroll
    for (int k = 0; k < 7; ++k) {
        const int ch = tid + 512 * k;
        const int mat = ch >= 1792, r = (ch - mat * 1792) >> 2, qt = ch & 3;
        wg[k] = *(const u32x4*)((mat ? w_up : w_gate) + (size_t)r * F_ + b * 32 + qt * 8);
    }
    if (tid < 448) {
#pragma unroll
        for (int k = 0; k < 4; ++k) wd[k] = *(const u32x4*)(w_down + (size_t)(tid + 448 * k) * D_ + b * 8);
    }
    const bool act = lane < 56;
    const int lo = act ? lane : 0;
    const u16* modp = (const u16*)(ws + WS_MOD);
    const u32x4 sbv = *(const u32x4*)(modp + 3 * D_ + lo * 8);
    const u32x4 cbv = *(const u32x4*)(modp + 4 * D_ + lo * 8);

    unsigned k4[4];
#pragma unroll
    for (int j = 0; j < 4; ++j) {
        const unsigned bits = (j & 1) ? (rv[j >> 1] >> 16) : (rv[j >> 1] & 0xffffu);
        const unsigned mono = (bits & 0x8000u) ? (~bits & 0xffffu) : (bits | 0x8000u);
        k4[j] = (mono << 16) | (unsigned)(2047 - (4 * tid + j));
    }
    unsigned* cand = (unsigned*)(smem + SM3_K);
    {
        unsigned mycand = 0;
#pragma unroll
        for (int it = 0; it < 8; ++it) {
            const unsigned m = wave_umax(umax_(umax_(k4[0], k4[1]), umax_(k4[2], k4[3])));
#pragma unroll
            for (int j = 0; j < 4; ++j) k4[j] = k4[j] == m ? 0u : k4[j];
            mycand = lane == it ? m : mycand;
        }
        if (lane < 8) cand[w * 8 + lane] = mycand;
    }
    __syncthreads();
    unsigned sel = 0;
    {
        unsigned kk = cand[lane];
#pragma unroll
        for (int it = 0; it < 8; ++it) {
            const unsigned m = wave_umax(kk);
            kk = kk == m ? 0u : kk;
            sel = it == w ? m : sel;
        }
    }
    sel = __builtin_amdgcn_readfirstlane(sel);
    const int tsel = 2047 - (int)(sel & 0xffffu);
    const u32x4 xv = *(const u32x4*)(x + (size_t)tsel * D_ + lo * 8);
    const u16* wcol = (const u16*)(ws + WS_W + (size_t)(tsel >> 3) * (D_ * 16)) + (tsel & 7) + lo * 64;
    u16 wv[8];
#pragma unroll
    for (int j = 0; j < 8; ++j) wv[j] = wcol[j * 8];
#pragma unroll
    for (int k = 0; k < 7; ++k) *(u32x4*)(smem + SM3_WG + (tid + 512 * k) * 16) = wg[k];
    if (tid < 448) {
#pragma unroll
        for (int k = 0; k < 4; ++k) *(u32x4*)(smem + SM3_WD + (tid + 448 * k) * 16) = wd[k];
    }
    float x1[8], h2[8];
#pragma unroll
    for (int j = 0; j < 4; ++j) {
        x1[2 * j] = bfr(bflo(xv[j]) + bf2f(wv[2 * j]));
        x1[2 * j + 1] = bfr(bfhi(xv[j]) + bf2f(wv[2 * j + 1]));
    }
    ln2_core(x1, act, sbv, cbv, h2);
    if (act) {
        u32x4 o;
#pragma unroll
        for (int j = 0; j < 4; ++j) o[j] = pk_bf16(h2[2 * j], h2[2 * j + 1]);
        *(u32x4*)(smem + SM3_H2 + w * 912 + lane * 16) = o;
    }
    float* keep = (float*)(smem + SM3_X1);
    if (lane == b) {
#pragma unroll
        for (int j = 0; j < 8; ++j) { keep[w * 8 + j] = x1[j]; keep[64 + w * 8 + j] = h2[j]; }
    }
    __syncthreads();
    if (lane == 0) cand[w] = sel;
    const int fr = lane & 15, fq = lane >> 4;
    const unsigned sbase = (unsigned)(size_t)smem;
    {
        const int mat = w & 1, ct = (w >> 1) & 1, kh = w >> 2;
        f32x4 acc = {0, 0, 0, 0};
        const unsigned wb = sbase + (mat ? SM3_WU : SM3_WG) + (8 * fq + (fr >> 2)) * 64 + ct * 32 + (fr & 3) * 8;
#pragma unroll
        for (int s = 0; s < 7; ++s) {
            const int ks = kh * 7 + s;
            const bf16x8 a = *(const bf16x8*)(smem + SM3_H2 + (fr & 7) * 912 + ks * 64 + fq * 16);
            const u32x2 b0 = tr_read(wb + ks * 2048), b1 = tr_read(wb + ks * 2048 + 256);
            const u32x4 bv = {b0[0], b0[1], b1[0], b1[1]};
            acc = MFMAB(a, __builtin_bit_cast(bf16x8, bv), acc);
        }
        float* P = (float*)(smem + SM3_P) + w * 128;
        if (fq < 2) {
#pragma unroll
            for (int j = 0; j < 4; ++j) P[(4 * fq + j) * 16 + fr] = acc[j];
        }
    }
    u32x4 bd[4];
    {
        const int ww = w < 7 ? w : 0;
        const unsigned wb = sbase + SM3_WD + (8 * fq + (fr >> 2)) * 32 + (fr & 3) * 8;
#pragma unroll
        for (int s = 0; s < 4; ++s) {
            const int ks = ww * 4 + s;
            const u32x2 b0 = tr_read(wb + ks * 1024), b1 = tr_read(wb + ks * 1024 + 128);
            bd[s] = (u32x4){b0[0], b0[1], b1[0], b1[1]};
        }
    }
    __syncthreads();
    if (tid < 64) {
        const int tok = tid >> 3, par = (tid >> 2) & 1, qd = tid & 3;
        const float* P = (const float*)(smem + SM3_P);
        unsigned hv[4];
#pragma unroll
        for (int i = 0; i < 4; ++i) {
            const int cc = 2 * (4 * qd + i) + par, ct = cc >> 4, cl = cc & 15;
            const float gs = P[(0 + 2 * ct) * 128 + tok * 16 + cl] + P[(4 + 2 * ct) * 128 + tok * 16 + cl];
            const float us = P[(1 + 2 * ct) * 128 + tok * 16 + cl] + P[(5 + 2 * ct) * 128 + tok * 16 + cl];
            const float gg = bfr(gs), uu = bfr(us);
            const float sg = bfr(gg / (1.f + expf(-gg)));
            unsigned bits = pk_bf16(sg * uu, 0.f) & 0xffffu;
            hv[i] = bits == 0xffffu ? 0x7fc0u : bits;
        }
        const unsigned long long v = (unsigned long long)(hv[0] | (hv[1] << 16)) | ((unsigned long long)(hv[2] | (hv[3] << 16)) << 32);
        GAS unsigned long long* dst = (GAS unsigned long long*)(ws + WS_A + ((par * 8 + tok) * 896 + 16 * b + 4 * qd) * 2);
        __hip_atomic_store(dst, v, __ATOMIC_RELAXED, __HIP_MEMORY_SCOPE_AGENT);
    }
    f32x4 acc = {0, 0, 0, 0};
    if (w < 7) {
        const __amdgpu_buffer_rsrc_t rs = __builtin_amdgcn_make_buffer_rsrc(ws + WS_A, 0, 2 * 8 * 896 * 2, 0x00020000);
        const unsigned off = (unsigned)((fr * 896 + w * 128 + fq * 8) * 2);
        u32x4 av[4];
        for (unsigned spins = 0; spins < (1u << 16); ++spins) {
            bool ok = true;
            asm volatile("" ::: "memory");
#pragma unroll
            for (int s = 0; s < 4; ++s) av[s] = __builtin_amdgcn_raw_buffer_load_b128(rs, off + s * 64, 0, 16);
#pragma unroll
            for (int s = 0; s < 4; ++s)
#pragma unroll
                for (int j = 0; j < 4; ++j)
                    ok = ok && ((av[s][j] & 0xffffu) != 0xffffu) && ((av[s][j] >> 16) != 0xffffu);
            if (__all(ok)) break;
            __builtin_amdgcn_s_sleep(1);
        }
#pragma unroll
        for (int s = 0; s < 4; ++s)
            acc = MFMAB(__builtin_bit_cast(bf16x8, av[s]), __builtin_bit_cast(bf16x8, bd[s]), acc);
    }
    __syncthreads();
    {
        float* P = (float*)(smem + SM3_P) + w * 128;
        if (w < 7 && (fq >> 1) == (fr >> 3)) {
#pragma unroll
            for (int j = 0; j < 4; ++j) P[(4 * fq + j) * 8 + (fr & 7)] = acc[j];
        }
    }
    __syncthreads();
    if (tid < 64) {
        const int tok = tid >> 3, col = tid & 7;
        const float* P = (const float*)(smem + SM3_P);
        float s = 0.f;
#pragma unroll
        for (int ww = 0; ww < 7; ++ww) s += P[ww * 128 + tok * 8 + col] + P[ww * 128 + 64 + tok * 8 + col];
        const unsigned key = cand[tok];
        const unsigned mono = key >> 16;
        const unsigned bits = (mono & 0x8000u) ? (mono & 0x7fffu) : (~mono & 0xffffu);
        const float rr = __uint_as_float(bits << 16);
        const float rs = bfr(1.f / (1.f + expf(-rr)));
        const float y2 = bfr(bfr(s) * rs);
        const float bb = bfr(keep[64 + tok * 8 + col] + y2);
        const float gbv = bf2f(modp[5 * D_ + b * 8 + col]);
        const float o = keep[tok * 8 + col] + bfr(gbv * bb);
        const int ts = 2047 - (int)(key & 0xffffu);
        out[(size_t)ts * D_ + b * 8 + col] = (u16)(pk_bf16(o, 0.f) & 0xffff);
    }
}

extern "C" void kernel_launch(void* const* d_in, const int* in_sizes, int n_in,
                              void* d_out, int out_size, void* d_ws, size_t ws_size,
                              hipStream_t stream) {
    const u16* A_u = (const u16*)d_in[0];
    const u16* A_re = (const u16*)d_in[1];
    const u16* A_im = (const u16*)d_in[2];
    const u16* B_re = (const u16*)d_in[3];
    const u16* B_im = (const u16*)d_in[4];
    const u16* C_re = (const u16*)d_in[5];
    const u16* C_im = (const u16*)d_in[6];
    const u16* log_dt = (const u16*)d_in[7];
    const u16* Dskip = (const u16*)d_in[8];
    const u16* w_r = (const u16*)d_in[10];
    const u16* w_gate = (const u16*)d_in[11];
    const u16* w_up = (const u16*)d_in[12];
    const u16* w_down = (const u16*)d_in[13];
    const u16* cvec = (const u16*)d_in[14];
    const u16* w_mod = (const u16*)d_in[15];
    unsigned char* ws = (unsigned char*)d_ws;
    u16* out = (u16*)d_out;
    k0_prep<<<NB_KG + NB_MOD + NB_LN, 256, 0, stream>>>(A_u, A_re, A_im, B_re, B_im, C_re, C_im, log_dt, cvec, w_mod, ws);
    k1_conv<<<224, 256, 0, stream>>>(Dskip, ws);
    k2_ln2<<<256, 512, 0, stream>>>(A_u, w_r, ws, out);
    k3_ffn<<<NB_FFN, 512, 0, stream>>>(A_u, w_gate, w_up, w_down, ws, out);
}
```

```cpp
#include <hip/hip_runtime.h>
#include <stdint.h>

typedef _Float16 half8 __attribute__((ext_vector_type(8)));
typedef _Float16 half2v __attribute__((ext_vector_type(2)));
typedef __bf16 bf16x8 __attribute__((ext_vector_type(8)));
typedef __bf16 bf16x2 __attribute__((ext_vector_type(2)));
typedef float f32x4 __attribute__((ext_vector_type(4)));
typedef unsigned u32x4 __attribute__((ext_vector_type(4)));
typedef unsigned u32x2 __attribute__((ext_vector_type(2)));
typedef unsigned short u16;
#if defined(__HIP_DEVICE_COMPILE__)
#define NOPK __attribute__((target("no-packed-fp32-ops")))
#else
#define NOPK
#endif
#define DEV NOPK __device__ __forceinline__

constexpr int L_ = 2048, D_ = 448, N_ = 256, F_ = 1792;

constexpr unsigned WS_E    = 0;
constexpr unsigned WS_XG   = 4096;
constexpr unsigned WS_W    = WS_XG + 224u * 2048u * 8u;
constexpr unsigned WS_H2   = WS_W + L_ * D_ * 2;
constexpr unsigned WS_PART = WS_H2 + L_ * D_ * 2;
constexpr unsigned WS_R    = WS_PART + 448u * 32u * 32u;
constexpr unsigned WS_DONE = WS_R + 4096;
constexpr unsigned WS_MOD2 = WS_DONE + 8192;
constexpr unsigned WS_END  = WS_MOD2 + 5376;

#define STAMP(i)

DEV float u2f(unsigned u) { return __builtin_bit_cast(float, u); }
DEV unsigned f2u(float f) { return __builtin_bit_cast(unsigned, f); }
DEV void bar() { asm volatile("s_waitcnt lgkmcnt(0)\n\ts_barrier" ::: "memory"); }
DEV void cfence() { asm volatile("" ::: "memory"); }
DEV bool wave_all(bool p) { return __builtin_amdgcn_ballot_w64(!p) == 0; }
DEV float bf2f(u16 v) { return u2f((unsigned)v << 16); }
DEV float bflo(unsigned v) { return u2f(v << 16); }
DEV float bfhi(unsigned v) { return u2f(v & 0xffff0000u); }
DEV unsigned pk_bf16(float a, float b) {
    bf16x2 v = {(__bf16)a, (__bf16)b};
    return __builtin_bit_cast(unsigned, v);
}
DEV float bfr(float x) { return bflo(pk_bf16(x, x)); }
DEV unsigned pk_f16(float a, float b) {
    half2v v = {(_Float16)a, (_Float16)b};
    return __builtin_bit_cast(unsigned, v);
}
DEV unsigned umax_(unsigned a, unsigned b) { return a > b ? a : b; }
DEV unsigned umin_(unsigned a, unsigned b) { return a < b ? a : b; }
template <int CTRL> DEV unsigned dpp_u(unsigned v) {
    return (unsigned)__builtin_amdgcn_update_dpp(0, (int)v, CTRL, 0xf, 0xf, true);
}
template <int CTRL> DEV float dpp_f(float v) { return u2f(dpp_u<CTRL>(f2u(v))); }
DEV float row_sum(float v) {
    v += dpp_f<0xB1>(v);
    v += dpp_f<0x4E>(v);
    v += dpp_f<0x141>(v);
    v += dpp_f<0x140>(v);
    return v;
}
DEV float fold32(float a, float b) {
    auto r = __builtin_amdgcn_permlane32_swap(f2u(a), f2u(b), false, false);
    return u2f(r[0]) + u2f(r[1]);
}
DEV float fold16(float a, float b) {
    auto r = __builtin_amdgcn_permlane16_swap(f2u(a), f2u(b), false, false);
    return u2f(r[0]) + u2f(r[1]);
}
DEV float rdlane(float v, int l) { return u2f((unsigned)__builtin_amdgcn_readlane((int)f2u(v), l)); }
DEV void wave_sum2(float a, float b, float& sa, float& sb) {
    float v = fold32(a, b);
    v = fold16(v, v);
    v = row_sum(v);
    sa = rdlane(v, 0); sb = rdlane(v, 32);
}
DEV float wave_sum(float a) {
    float v = fold32(a, a);
    v = fold16(v, v);
    return row_sum(v);
}
DEV unsigned wave_umax(unsigned v) {
    v = umax_(v, dpp_u<0xB1>(v));
    v = umax_(v, dpp_u<0x4E>(v));
    v = umax_(v, dpp_u<0x141>(v));
    v = umax_(v, dpp_u<0x140>(v));
    { auto r = __builtin_amdgcn_permlane16_swap(v, v, false, false); v = umax_(r[0], r[1]); }
    { auto r = __builtin_amdgcn_permlane32_swap(v, v, false, false); v = umax_(r[0], r[1]); }
    return v;
}
DEV int exp_above(float m) {
    int e = (int)((f2u(m) >> 23) & 0xff) - 126;
    return e < -100 ? -100 : (e > 100 ? 100 : e);
}
DEV float pow2i(int e) { return u2f((unsigned)(e + 127) << 23); }
DEV float fexp(float x) { return __builtin_amdgcn_exp2f(x * 1.44269504088896341f); }
DEV float frcp(float x) { return __builtin_amdgcn_rcpf(x); }
DEV bool valid4(u32x4 v) { return umax_(umax_(v[0], v[1]), umax_(v[2], v[3])) != 0xffffffffu; }

typedef __amdgpu_buffer_rsrc_t rsrc_t;
DEV u32x4 ld16(rsrc_t r, unsigned off) { return __builtin_amdgcn_raw_buffer_load_b128(r, off, 0, 16); }
DEV u32x2 ld8(rsrc_t r, unsigned off) { return __builtin_amdgcn_raw_buffer_load_b64(r, off, 0, 16); }
DEV unsigned ld4(rsrc_t r, unsigned off) { return __builtin_amdgcn_raw_buffer_load_b32(r, off, 0, 16); }
DEV void st16(rsrc_t r, unsigned off, u32x4 v) { __builtin_amdgcn_raw_buffer_store_b128(v, r, off, 0, 16); }
DEV void st8(rsrc_t r, unsigned off, u32x2 v) { __builtin_amdgcn_raw_buffer_store_b64(v, r, off, 0, 16); }
DEV void st4(rsrc_t r, unsigned off, unsigned v) { __builtin_amdgcn_raw_buffer_store_b32(v, r, off, 0, 16); }
DEV void st2(rsrc_t r, unsigned off, unsigned v) { __builtin_amdgcn_raw_buffer_store_b16((u16)v, r, off, 0, 16); }
constexpr unsigned SPIN_CAP = 1u << 15;

constexpr int IMG_R1 = 0, IMG_RX = 560, IMG_RC = 608, IMG_RD = 864;
constexpr int lq_of(int l) {
    if (l < 256) return l;
    int s = l < 512 ? 1 : (l < 1024 ? 2 : 3);
    int q = l >> s, rem = l & ((1 << s) - 1), half = 1 << (s - 1);
    if (rem > half || (rem == half && (q & 1))) q++;
    return q << s;
}
constexpr int slot_of(int c) {
    if (c < 256) return (c & 15) * 16 + (c >> 4);
    if (c < 512) return (16 + ((c & 15) >> 1)) * 16 + ((c - 256) >> 4);
    if (c < 1024) return (24 + 4 * ((c - 512) >> 8) + ((c & 15) >> 2)) * 16 + ((c & 255) >> 4);
    if (c < 2048) return (32 + 2 * ((c - 1024) >> 8) + ((c & 15) >> 3)) * 16 + ((c & 255) >> 4);
    return 40 * 16;
}
struct KTab { u16 v[1032]; };
constexpr KTab make_ktab() {
    KTab t{};
    for (int n = 0; n < 1032; ++n) t.v[n] = 2 * 768;
    for (int n = 0; n < 560; ++n) { int l = 543 - n; if (l >= 0 && l <= 514) t.v[IMG_R1 + n] = (u16)(2 * slot_of(lq_of(l))); }
    for (int n = 0; n < 48; ++n) { int l = 1055 - n; if (l == 1027 || l == 1028) t.v[IMG_RX + n] = (u16)(2 * slot_of(1024)); }
    const int coffC[4] = {16, 4, 8, 12};
    for (int e = 0; e < 4; ++e)
        for (int n = 0; n < 64; ++n) { int lam = 79 - n; if (lam >= 32 && lam <= 63) t.v[IMG_RC + e * 64 + n] = (u16)(2 * slot_of(16 * lam + coffC[e])); }
    const int coffD[2] = {16, 8};
    for (int e = 0; e < 2; ++e)
        for (int n = 0; n < 80; ++n) { int lam = 127 - n; if (lam >= 64 && lam <= 127) t.v[IMG_RD + e * 80 + n] = (u16)(2 * slot_of(16 * lam + coffD[e])); }
    return t;
}
__device__ __attribute__((aligned(16))) const KTab g_ktab = make_ktab();

struct cf { float r, i; };
DEV cf cmul(cf a, cf b) { return {a.r * b.r - a.i * b.i, a.r * b.i + a.i * b.r}; }
DEV cf csqr(cf a) { return {a.r * a.r - a.i * a.i, (a.r + a.r) * a.i}; }
DEV cf cexp_pow2(float t2, float rh, float rl, float Lf) {
    float mag = __builtin_amdgcn_exp2f(t2 * Lf);
    float a = rh * Lf;
    float ph = (a - __builtin_rintf(a)) + rl * Lf;
    return {mag * __builtin_amdgcn_cosf(ph), mag * __builtin_amdgcn_sinf(ph)};
}
#define MFMA16(a, b, c) __builtin_amdgcn_mfma_f32_16x16x32_f16(a, b, c, 0, 0, 0)
#define MFMAB(a, b, c) __builtin_amdgcn_mfma_f32_16x16x32_bf16(a, b, c, 0, 0, 0)

DEV void ln_stats(const float (&f)[8], bool act, float& mb, float& rinv) {
    const float pv = u2f((unsigned)__builtin_amdgcn_readfirstlane((int)f2u(f[0])));
    float s1 = 0.f, s2 = 0.f;
#pragma unroll
    for (int j = 0; j < 8; ++j) { const float dd = f[j] - pv; s1 += dd; s2 = __builtin_fmaf(dd, dd, s2); }
    s1 = act ? s1 : 0.f; s2 = act ? s2 : 0.f;
    float t1, t2;
    wave_sum2(s1, s2, t1, t2);
    const float dm = t1 * (1.f / D_);
    const float var = __builtin_fmaxf(__builtin_fmaf(-dm, dm, t2 * (1.f / D_)), 0.f);
    mb = bfr(pv + dm);
    rinv = frcp(bfr(__builtin_amdgcn_sqrtf(bfr(bfr(var) + 1e-5f))));
}

constexpr int KG_ROW = 288, KG_BUF = 16 * KG_ROW, KG_WAVE = 2 * KG_BUF;
constexpr int KC_STR = 2112;
constexpr int U_PAD = 256, U_LEN = U_PAD + L_;
constexpr int SM_KG = 0;
constexpr int SM_T16 = 73728;
constexpr int SM_KC = 76928;
constexpr int SM_U = 93824;
constexpr int SM_TILE = 103040;
constexpr int SM_PRM = 110336;
constexpr int SM_MISC = 115712;
constexpr int SM_M2 = 116736;
constexpr int SM_TOTAL = SM_M2 + 12544;
#define LAS __attribute__((address_space(3)))
typedef short s16x4 __attribute__((ext_vector_type(4)));
DEV u32x2 tr_read(const unsigned char* p) {
    return __builtin_bit_cast(u32x2, __builtin_amdgcn_ds_read_tr16_b64_v4i16((LAS s16x4*)p));
}
DEV half8 kfrag(const unsigned char* pa, const unsigned char* pb) {
    const u32x2 a = *(const u32x2*)pa, b = *(const u32x2*)pb;
    const u32x4 v = {a[0], a[1], b[0], b[1]};
    return __builtin_bit_cast(half8, v);
}
DEV half8 box_frag(int p, int koff, int phif, int wd) {
    int sh = p - phif - koff - (wd - 1);
    sh = sh > 31 ? 31 : sh;
    const unsigned m = (unsigned)(((unsigned long long)((1u << wd) - 1u) << (sh + 32)) >> 32);
    u32x4 v;
#pragma unroll
    for (int k = 0; k < 4; ++k) {
        const unsigned x = (m >> (2 * k)) & 3u;
        v[k] = ((x | (x << 15)) & 0x10001u) * 0x3C00u;
    }
    return __builtin_bit_cast(half8, v);
}
DEV half8 pack_v(f32x4 a, f32x4 b) {
    u32x4 v = {pk_f16(a[0], a[1]), pk_f16(a[2], a[3]), pk_f16(b[0], b[1]), pk_f16(b[2], b[3])};
    return __builtin_bit_cast(half8, v);
}

template <int HS, int TG>
DEV void conv_mfma(const unsigned char* Uc, const unsigned char* kb1, const unsigned char* kb2,
                   half8 bx0, half8 bx1, half8 bx2, half8 bx3, f32x4* red) {
    constexpr int I0 = TG ? 5 : 0, I1 = TG ? 7 : 4, NT = I1 - I0 + 1;
    constexpr int C0 = TG ? 1 : 0, C1 = TG ? 5 : 2;
    constexpr int D1 = TG ? 3 : 0;
    const f32x4 zero4 = {0, 0, 0, 0};
    f32x4 acc[NT];
#pragma unroll
    for (int i = 0; i < NT; ++i) acc[i] = zero4;
    half8 fc[6], fd[4];
#define UFRAG(delta) (*(const half8*)(Uc + 64 * (delta)))
#define ACC(I, a, b) do { if ((I) >= I0 && (I) <= I1) acc[(I) - I0] = MFMA16(a, b, acc[(I) - I0]); } while (0)
    if (HS == 0) {
        constexpr int M0 = TG ? 1 : 0, M1 = I1;
        half8 ub[8];
#pragma unroll
        for (int m = M0; m <= M1; ++m) ub[m] = UFRAG(8 * m);
        const half8 k0 = kfrag(kb1 + 64 * 16, kb1 + 64 * 16 + 8), k8 = kfrag(kb1 + 64 * 8, kb1 + 64 * 8 + 8);
        const half8 k16 = kfrag(kb1, kb1 + 8), kx = kfrag(kb1 + 2 * IMG_RX, kb1 + 2 * IMG_RX + 8);
#pragma unroll
        for (int m = C0; m <= C1; ++m) fc[m] = pack_v(MFMA16(ub[m], bx0, zero4), MFMA16(ub[m], bx1, zero4));
#pragma unroll
        for (int m = M0; m <= M1; ++m) {
            ACC(m, k0, ub[m]); ACC(m + 1, k8, ub[m]); ACC(m + 2, k16, ub[m]); ACC(m + 4, kx, ub[m]);
        }
    } else {
        half8 ua[6], ub[4];
#pragma unroll
        for (int m = 0; m <= C1; ++m) ua[m] = *(const half8*)(Uc + 64 * 8 * m - 16);
#pragma unroll
        for (int m = 0; m <= D1; ++m) ub[m] = UFRAG(8 * m);
#pragma unroll
        for (int m = C0; m <= C1; ++m) fc[m] = pack_v(MFMA16(ua[m], bx0, zero4), MFMA16(ua[m], bx1, zero4));
#pragma unroll
        for (int m = 0; m <= D1; ++m) fd[m] = pack_v(MFMA16(ua[m], bx2, zero4), MFMA16(ub[m], bx3, zero4));
    }
    constexpr int MM0 = TG ? 4 : 0;
#pragma unroll
    for (int rr = (HS == 0 ? 1 : 0); rr < 4; ++rr) {
        const int r = 4 * HS + rr;
        half8 ub[8];
#pragma unroll
        for (int m = MM0; m <= I1; ++m) ub[m] = UFRAG(8 * m - r);
        const half8 ka = kfrag(kb1 + 64 * (16 - r), kb1 + 64 * (16 - r) + 8);
        const half8 kb = kfrag(kb1 + 64 * (8 - r), kb1 + 64 * (8 - r) + 8);
#pragma unroll
        for (int m = MM0; m <= I1; ++m) { ACC(m, ka, ub[m]); ACC(m + 1, kb, ub[m]); }
    }
    {
        constexpr int ea = HS ? 0 : 1, eb = HS ? 3 : 2;
#pragma unroll
        for (int s = 2; s <= 4; ++s) {
            const half8 kc = kfrag(kb2 + 2 * (IMG_RC + ea * 64 + 16 * (4 - s)), kb2 + 2 * (IMG_RC + eb * 64 + 16 * (4 - s)));
#pragma unroll
            for (int m = C0; m <= C1; ++m) ACC(m + s, fc[m], kc);
        }
    }
    if (HS == 1) {
#pragma unroll
        for (int s = 4; s <= I1; ++s) {
            const half8 kd = kfrag(kb2 + 2 * (IMG_RD + 16 * (7 - s)), kb2 + 2 * (IMG_RD + 80 + 16 * (7 - s)));
#pragma unroll
            for (int m = 0; m <= D1; ++m) ACC(m + s, fd[m], kd);
        }
    }
#undef UFRAG
#undef ACC
#pragma unroll
    for (int i = 0; i < NT; ++i) red[64 * (I0 + i)] = acc[i];
}

NOPK __global__ __launch_bounds__(512, 2) void fused_block(
    const u16* __restrict__ x, const u16* __restrict__ A_re, const u16* __restrict__ A_im,
    const u16* __restrict__ B_re, const u16* __restrict__ B_im, const u16* __restrict__ C_re,
    const u16* __restrict__ C_im, const u16* __restrict__ log_dt, const u16* __restrict__ Dskip,
    const u16* __restrict__ w_r, const u16* __restrict__ w_gate, const u16* __restrict__ w_up,
    const u16* __restrict__ w_down, const u16* __restrict__ cvec, const u16* __restrict__ w_mod,
    unsigned char* __restrict__ ws, u16* __restrict__ out)
{
    __shared__ __attribute__((aligned(16))) unsigned char smem[SM_TOTAL];
    const int tid = threadIdx.x, lane = tid & 63;
    const int w = __builtin_amdgcn_readfirstlane(tid >> 6);
    const int b = blockIdx.x, xg = b & 7, jj = b >> 3;
    const int g = xg * 32 + jj;
    const bool is_conv = jj < 28;
    const int pair = xg * 28 + jj;
    const int fb = xg * 4 + (jj - 28);
    const int t = 8 * g + w;
    const bool act = lane < 56;
    const int lo = act ? lane : 0;
    const int fr = lane & 15, fq = lane >> 4;
    const rsrc_t rs = __builtin_amdgcn_make_buffer_rsrc(ws, 0, WS_END, 0x00020000);
    const rsrc_t ro = __builtin_amdgcn_make_buffer_rsrc(out, 0, L_ * D_ * 2, 0x00020000);
    float* misc = (float*)(smem + SM_MISC);
    STAMP(0);

    const u32x4 xv = *(const u32x4*)(x + (size_t)t * D_ + lo * 8);
    const unsigned tag = ld4(rs, WS_E) + 1u;
    u16 p_are = 0, p_aim = 0, p_bre = 0, p_bim = 0, p_cre = 0, p_cim = 0, p_ldt = 0;
    u32x2 kix0 = {0, 0}, kix1 = {0, 0};
    if (is_conv) {
        const int pi = pair * 512 + tid;
        p_are = A_re[pi]; p_aim = A_im[pi]; p_bre = B_re[pi]; p_bim = B_im[pi]; p_cre = C_re[pi]; p_cim = C_im[pi];
        p_ldt = log_dt[2 * pair + (tid >> 8)];
        kix0 = *(const u32x2*)(&g_ktab.v[4 * (tid & 255)]);
        kix1 = *(const u32x2*)(&g_ktab.v[4 * (tid & 255) + 4]);
    }
    const u32x4 wrv = *(const u32x4*)(w_r + lo * 8);
    cfence();
    {
        const u32x4 ones = {0xffffffffu, 0xffffffffu, 0xffffffffu, 0xffffffffu}, zer = {0, 0, 0, 0};
        if (tid < 448) {
            const unsigned wo = is_conv ? (unsigned)(g * 7168 + tid * 16)
                                        : (unsigned)(((g >> 2) * 224 + (tid >> 1)) * 128 + (g & 3) * 32 + (tid & 1) * 16);
            st16(rs, WS_W + wo, ones);
            st16(rs, WS_H2 + g * 7168 + tid * 16, ones);
            if (tid < 112) st16(rs, WS_PART + g * 1792 + tid * 16, ones);
        } else if (tid < 451) {
            st16(rs, tid == 448 ? WS_R + g * 16 : WS_DONE + g * 32 + (tid - 449) * 16, zer);
        }
    }
    cfence();
    {
        float f[8];
#pragma unroll
        for (int j = 0; j < 4; ++j) { f[2 * j] = bflo(xv[j]); f[2 * j + 1] = bfhi(xv[j]); }
        float mb, rinv;
        ln_stats(f, act, mb, rinv);
        u32x4 o;
#pragma unroll
        for (int j = 0; j < 4; ++j) {
            const unsigned dd = pk_bf16(f[2 * j] - mb, f[2 * j + 1] - mb);
            o[j] = pk_bf16(bflo(dd) * rinv, bfhi(dd) * rinv);
        }
        if (act) *(u32x4*)(smem + SM_TILE + (w * 228 + lane * 4) * 4) = o;
    }
    STAMP(1);
    asm volatile("s_waitcnt vmcnt(0)" ::: "memory");
    bar();
    {
        const unsigned* tile = (const unsigned*)(smem + SM_TILE);
#pragma unroll
        for (int it = 0; it < 2; ++it) {
            const int e = tid + 512 * it;
            if (e < 896) {
                const int pr = e >> 2, k = e & 3;
                const u32x4 v = {tile[(2 * k) * 228 + pr], tag, tile[(2 * k + 1) * 228 + pr], tag};
                st16(rs, WS_XG + pr * 16384 + (8 * g + 2 * k) * 8, v);
            }
        }
    }
    STAMP(2);
    cfence();

    u32x4 fgu[14], fdn[8];
    if (is_conv) {
        const int c = w >> 2;
        unsigned wm0 = 0, wm1 = 0, wm2 = 0;
        u16 cval = 0;
        if (tid < 448) {
            const u16* wp = w_mod + (size_t)tid * (6 * D_) + 2 * pair;
            wm0 = *(const unsigned*)wp; wm1 = *(const unsigned*)(wp + D_); wm2 = *(const unsigned*)(wp + 2 * D_);
            cval = cvec[tid];
        }
        const unsigned dsk2 = *(const unsigned*)(Dskip + 2 * pair);
        {
            const float dt = bfr(expf(bf2f(p_ldt)));
            float zr = dt * bf2f(p_are);
            const float zi = dt * bf2f(p_aim);
            const float dbr = dt * bf2f(p_bre), dbi = dt * bf2f(p_bim);
            const float cre = bf2f(p_cre), cim = bf2f(p_cim);
            cf cb = {cre * dbr - cim * dbi, cre * dbi + cim * dbr};
            float mx, rho;
            {
                const unsigned a = f2u(__builtin_fmaxf(__builtin_fabsf(cb.r), __builtin_fabsf(cb.i)));
                const unsigned bb = f2u(__builtin_fmaxf(zr, 0.f));
                auto r = __builtin_amdgcn_permlane32_swap(a, bb, false, false);
                unsigned v = umax_(r[0], r[1]);
                { auto q = __builtin_amdgcn_permlane16_swap(v, v, false, false); v = umax_(q[0], q[1]); }
                v = umax_(v, dpp_u<0xB1>(v)); v = umax_(v, dpp_u<0x4E>(v));
                v = umax_(v, dpp_u<0x141>(v)); v = umax_(v, dpp_u<0x140>(v));
                mx = rdlane(u2f(v), 0); rho = rdlane(u2f(v), 32);
            }
            STAMP(22);
            zr -= rho;
            const int ew = exp_above(mx);
            const float sc = pow2i(14 - ew);
            cb.r *= sc; cb.i *= sc;
            const float t2 = zr * 1.44269504088896341f;
            const float rh = zi * 0.15915494309189535f;
            const float rl = __builtin_fmaf(zi, 0.15915494309189535f, -rh) + zi * 6.4206382e-09f;
            unsigned char* wb = smem + SM_KG + w * KG_WAVE;
            unsigned* st0 = (unsigned*)(wb + lane * 4);
            unsigned* st1 = (unsigned*)(wb + KG_BUF + lane * 4);
            const unsigned char* rd0 = wb + fr * KG_ROW + fq * 16;
            const unsigned char* rd1 = rd0 + KG_BUF;
#define ST(buf, row, v) buf[(row) * (KG_ROW / 4)] = pk_f16((v).r, (v).i)
#define STC(buf, row, v) buf[(row) * (KG_ROW / 4)] = pk_f16((v).r, -(v).i)
            {
                cf s1 = cexp_pow2(t2, rh, rl, 16.f), s2 = csqr(s1), s4 = csqr(s2), s8 = csqr(s4);
                cf q0 = cb, q1 = cmul(q0, s1), q2 = cmul(q0, s2), q3 = cmul(q1, s2);
                cf q4 = cmul(q0, s4), q5 = cmul(q1, s4), q6 = cmul(q2, s4), q7 = cmul(q3, s4);
                STC(st0, 0, q0); STC(st0, 1, q1); STC(st0, 2, q2); STC(st0, 3, q3);
                STC(st0, 4, q4); STC(st0, 5, q5); STC(st0, 6, q6); STC(st0, 7, q7);
                cf tt;
                tt = cmul(q0, s8); STC(st0, 8, tt);  tt = cmul(q1, s8); STC(st0, 9, tt);
                tt = cmul(q2, s8); STC(st0, 10, tt); tt = cmul(q3, s8); STC(st0, 11, tt);
                tt = cmul(q4, s8); STC(st0, 12, tt); tt = cmul(q5, s8); STC(st0, 13, tt);
                tt = cmul(q6, s8); STC(st0, 14, tt); tt = cmul(q7, s8); STC(st0, 15, tt);
            }
            cf p1 = cexp_pow2(t2, rh, rl, 1.f), p2 = csqr(p1), p3 = cmul(p2, p1), p4 = csqr(p2);
            cf p5 = cmul(p4, p1), p6 = cmul(p4, p2), p7 = cmul(p4, p3), p8 = csqr(p4);
            cf p10 = cmul(p8, p2), p12 = cmul(p8, p4), p14 = cmul(p8, p6);
            {
                cf one = {1.f, 0.f}, tt;
                ST(st1, 0, one); ST(st1, 1, p1); ST(st1, 2, p2); ST(st1, 3, p3);
                ST(st1, 4, p4); ST(st1, 5, p5); ST(st1, 6, p6); ST(st1, 7, p7);
                ST(st1, 8, p8); tt = cmul(p8, p1); ST(st1, 9, tt); ST(st1, 10, p10); tt = cmul(p8, p3); ST(st1, 11, tt);
                ST(st1, 12, p12); tt = cmul(p8, p5); ST(st1, 13, tt); ST(st1, 14, p14); tt = cmul(p8, p7); ST(st1, 15, tt);
            }
            cfence();
            STAMP(21);
            half8 xf[4];
            f32x4 acc0 = {0, 0, 0, 0}, acc1 = {0, 0, 0, 0}, acc2 = {0, 0, 0, 0};
#pragma unroll
            for (int s = 0; s < 4; ++s) xf[s] = *(const half8*)(rd0 + s * 64);
#pragma unroll
            for (int s = 0; s < 4; ++s) acc0 = MFMA16(xf[s], *(const half8*)(rd1 + s * 64), acc0);
            cfence();
            {
                cf g1 = cexp_pow2(t2, rh, rl, 256.f), g2 = csqr(g1), g3 = cmul(g2, g1), g4 = csqr(g2);
                cf tt;
                ST(st0, 0, g1); tt = cmul(g1, p2); ST(st0, 1, tt); tt = cmul(g1, p4); ST(st0, 2, tt); tt = cmul(g1, p6); ST(st0, 3, tt);
                tt = cmul(g1, p8); ST(st0, 4, tt); tt = cmul(g1, p10); ST(st0, 5, tt); tt = cmul(g1, p12); ST(st0, 6, tt); tt = cmul(g1, p14); ST(st0, 7, tt);
                ST(st0, 8, g2); tt = cmul(g2, p4); ST(st0, 9, tt); tt = cmul(g2, p8); ST(st0, 10, tt); tt = cmul(g2, p12); ST(st0, 11, tt);
                ST(st0, 12, g3); tt = cmul(g3, p4); ST(st0, 13, tt); tt = cmul(g3, p8); ST(st0, 14, tt); tt = cmul(g3, p12); ST(st0, 15, tt);
                cf g5 = cmul(g4, g1), g6 = cmul(g4, g2), g7 = cmul(g4, g3), g8 = csqr(g4);
                ST(st1, 0, g4); tt = cmul(g4, p8); ST(st1, 1, tt);
                ST(st1, 2, g5); tt = cmul(g5, p8); ST(st1, 3, tt);
                ST(st1, 4, g6); tt = cmul(g6, p8); ST(st1, 5, tt);
                ST(st1, 6, g7); tt = cmul(g7, p8); ST(st1, 7, tt);
                ST(st1, 8, g8);
            }
            cfence();
#pragma unroll
            for (int s = 0; s < 4; ++s) {
                acc1 = MFMA16(xf[s], *(const half8*)(rd0 + s * 64), acc1);
                acc2 = MFMA16(xf[s], *(const half8*)(rd1 + s * 64), acc2);
            }
            cfence();
#undef ST
#undef STC
            const float us = pow2i(ew - 14);
            acc0 *= us; acc1 *= us; acc2 *= us;
            if (rho > 0.f) {
                const float rw = rho * 1.44269504088896341f;
                const int h1 = fr < 8 ? 256 + 2 * fr : 512 + 256 * ((fr - 8) >> 2) + 4 * (fr & 3);
                const int h2 = fr < 8 ? 1024 + 256 * (fr >> 1) + 8 * (fr & 1) : 2048;
#pragma unroll
                for (int j = 0; j < 4; ++j) {
                    const int lo16 = 16 * (4 * fq + j);
                    acc0[j] *= exp2f(rw * (float)(fr + lo16));
                    acc1[j] *= exp2f(rw * (float)(h1 + lo16));
                    acc2[j] *= exp2f(rw * (float)(h2 + lo16 > 2048 ? 2048 : h2 + lo16));
                }
            }
            float* P = (float*)wb;
            *(f32x4*)(P + (0 + fr) * 20 + 4 * fq) = acc0;
            *(f32x4*)(P + (16 + fr) * 20 + 4 * fq) = acc1;
            *(f32x4*)(P + (32 + fr) * 20 + 4 * fq) = acc2;
            const float eb = (float)ew + __builtin_fminf(__builtin_ceilf(rho * (2048.f * 1.44269504088896341f)), 100.f);
            if (lane == 0) misc[w] = eb;
        }
        STAMP(20);
        {
            const float cvf = bf2f(cval);
            const float s = bfr(cvf * frcp(1.f + fexp(-cvf)));
            const float a = fold32(s * bflo(wm0), s * bfhi(wm0));
            const float bb = fold32(s * bflo(wm1), s * bfhi(wm1));
            const float cc = fold32(s * bflo(wm2), s * bfhi(wm2));
            const float d = row_sum(fold16(a, bb));
            const float e = row_sum(fold16(cc, cc));
            if (fr == 0) { misc[16 + w * 8 + fq] = d; misc[16 + w * 8 + 4 + fq] = e; }
        }
        STAMP(3);
        bar();
        STAMP(23);
        int eM0, eM1;
        {
            const f32x4 m0 = *(const f32x4*)misc, m1 = *(const f32x4*)(misc + 4);
            eM0 = (int)__builtin_fminf(__builtin_fmaxf(__builtin_fmaxf(m0[0], m0[1]), __builtin_fmaxf(m0[2], m0[3])), 110.f);
            eM1 = (int)__builtin_fminf(__builtin_fmaxf(__builtin_fmaxf(m1[0], m1[1]), __builtin_fmaxf(m1[2], m1[3])), 110.f);
        }
        {
            const int th = tid & 255;
            unsigned char* T16 = smem + SM_T16 + c * 1600;
            if (th < 192) {
                const float* P = (const float*)(smem + SM_KG + c * 4 * KG_WAVE) + (th >> 2) * 20 + 4 * (th & 3);
                f32x4 s = *(const f32x4*)P + *(const f32x4*)(P + KG_WAVE / 4);
                s += *(const f32x4*)(P + 2 * (KG_WAVE / 4)) + *(const f32x4*)(P + 3 * (KG_WAVE / 4));
                s *= pow2i(6 - (c ? eM1 : eM0));
                *(u32x2*)(T16 + th * 8) = (u32x2){pk_f16(s[0], s[1]), pk_f16(s[2], s[3])};
            } else if (th == 192) {
                *(unsigned*)(T16 + 1536) = 0;
            } else if (tid >= 504) {
                const int i = tid - 504;
                float s = 0.f;
#pragma unroll
                for (int ww = 0; ww < 7; ++ww) s += misc[16 + ww * 8 + i];
                misc[80 + i] = bfr(s);
            }
            if (tid < 128) *(u32x2*)(smem + SM_U + (tid >> 6) * (U_LEN * 2) + (tid & 63) * 8) = (u32x2){0, 0};
        }
        bar();
        STAMP(24);
        {
            const unsigned char* T16 = smem + SM_T16 + c * 1600;
            const unsigned e0 = *(const u16*)(T16 + (kix0[0] & 0xffff)), e1 = *(const u16*)(T16 + (kix0[0] >> 16));
            const unsigned e2 = *(const u16*)(T16 + (kix0[1] & 0xffff)), e3 = *(const u16*)(T16 + (kix0[1] >> 16));
            const unsigned e4 = *(const u16*)(T16 + (kix1[0] & 0xffff)), e5 = *(const u16*)(T16 + (kix1[0] >> 16));
            const unsigned e6 = *(const u16*)(T16 + (kix1[1] & 0xffff));
            const unsigned p01 = e0 | (e1 << 16), p12 = e1 | (e2 << 16), p23 = e2 | (e3 << 16);
            const unsigned p34 = e3 | (e4 << 16), p45 = e4 | (e5 << 16), p56 = e5 | (e6 << 16);
            unsigned char* kc = smem + SM_KC + c * 4 * KC_STR + (tid & 255) * 8;
            *(u32x2*)(kc) = (u32x2){p01, p23};
            *(u32x2*)(kc + KC_STR) = (u32x2){p12, p34};
            *(u32x2*)(kc + 2 * KC_STR) = (u32x2){p23, p45};
            *(u32x2*)(kc + 3 * KC_STR) = (u32x2){p34, p56};
        }
        const int hs = w & 1, tg = (w >> 1) & 1;
        const int koffb = -16 * (fq >> 1) + 8 * (fq & 1);
        half8 bx0, bx1, bx2, bx3;
        if (hs == 0) { bx0 = box_frag(fr, koffb, 3, 3); bx1 = box_frag(fr, koffb, 6, 5); bx2 = bx0; bx3 = bx0; }
        else {
            bx0 = box_frag(fr, koffb - 8, 14, 5); bx1 = box_frag(fr, koffb - 8, 11, 3);
            bx2 = box_frag(fr, koffb - 8, 12, 9); bx3 = box_frag(fr, koffb, 5, 7);
        }
        const f32x4 mA = *(const f32x4*)(misc + 80), mB = *(const f32x4*)(misc + 84);
        const float opc0 = bfr(1.f + mA[1]), opc1 = bfr(1.f + mA[3]);
        const int eu0 = exp_above(32.f * __builtin_fabsf(opc0) + __builtin_fabsf(mA[0]));
        const int eu1 = exp_above(32.f * __builtin_fabsf(opc1) + __builtin_fabsf(mA[2]));
        const float su0 = pow2i(11 - eu0), su1 = pow2i(11 - eu1), iu0 = pow2i(eu0 - 11), iu1 = pow2i(eu1 - 11);
        const float oc0 = opc0 * su0, oc1 = opc1 * su1, sa0 = mA[0] * su0, sa1 = mA[2] * su1;
        const float dk0 = bflo(dsk2) * iu0, dk1 = bfhi(dsk2) * iu1;
        STAMP(4);
        u32x4 g0, g1;
        for (unsigned spins = 0; spins < SPIN_CAP; ++spins) {
            cfence();
            g0 = ld16(rs, WS_XG + pair * 16384 + tid * 32);
            g1 = ld16(rs, WS_XG + pair * 16384 + tid * 32 + 16);
            if (wave_all(((g0[1] ^ tag) | (g0[3] ^ tag) | (g1[1] ^ tag) | (g1[3] ^ tag)) == 0)) break;
            __builtin_amdgcn_s_sleep(2);
        }
        STAMP(5);
        unsigned hd[4];
        {
            const unsigned xn[4] = {g0[0], g0[2], g1[0], g1[2]};
            float h0[4], h1[4];
#pragma unroll
            for (int j = 0; j < 4; ++j) {
                const unsigned a = pk_bf16(bflo(xn[j]) * oc0, bfhi(xn[j]) * oc1);
                const unsigned h = pk_bf16(bflo(a) + sa0, bfhi(a) + sa1);
                h0[j] = bflo(h); h1[j] = bfhi(h);
            }
            unsigned char* U0 = smem + SM_U + (U_PAD + 4 * tid) * 2;
            *(u32x2*)(U0) = (u32x2){pk_f16(h0[0], h0[1]), pk_f16(h0[2], h0[3])};
            *(u32x2*)(U0 + U_LEN * 2) = (u32x2){pk_f16(h1[0], h1[1]), pk_f16(h1[2], h1[3])};
#pragma unroll
            for (int j = 0; j < 4; ++j) hd[j] = pk_bf16(h0[j] * dk0, h1[j] * dk1);
        }
        bar();
        STAMP(6);
        if (b == 0 && tid == 0) st4(rs, WS_E, tag);
        {
            const unsigned char* Uc = smem + SM_U + c * (U_LEN * 2) + (U_PAD + 16 * fr + koffb) * 2;
            const int sft = (3 - fr) & 3;
            const unsigned char* kcb = smem + SM_KC + c * 4 * KC_STR + sft * KC_STR;
            const unsigned char* kb1 = kcb + ((31 + koffb - fr) & ~3) * 2;
            const unsigned char* kb2 = kcb + ((15 - fr + 4 * fq) & ~3) * 2;
            f32x4* red = (f32x4*)(smem + SM_KG) + (c * 2 + hs) * 512 + 4 * fr + (fq ^ ((fr >> 1) & 3));
            if (hs == 0) { if (tg == 0) conv_mfma<0, 0>(Uc, kb1, kb2, bx0, bx1, bx2, bx3, red); else conv_mfma<0, 1>(Uc, kb1, kb2, bx0, bx1, bx2, bx3, red); }
            else         { if (tg == 0) conv_mfma<1, 0>(Uc, kb1, kb2, bx0, bx1, bx2, bx3, red); else conv_mfma<1, 1>(Uc, kb1, kb2, bx0, bx1, bx2, bx3, red); }
        }
        STAMP(7);
        bar();
        {
            const f32x4* red = (const f32x4*)(smem + SM_KG) + ((tid & ~3) | ((tid ^ (tid >> 3)) & 3));
            const f32x4 y0 = (red[0] + red[512]) * (pow2i(eM0 - 6) * iu0);
            const f32x4 y1 = (red[1024] + red[1536]) * (pow2i(eM1 - 6) * iu1);
            u32x4 o;
#pragma unroll
            for (int j = 0; j < 4; ++j) {
                const unsigned y = pk_bf16(y0[j], y1[j]);
                const unsigned z = pk_bf16(bflo(y) + bflo(hd[j]), bfhi(y) + bfhi(hd[j]));
                o[j] = umin_(pk_bf16(mB[0] * bflo(z), mB[2] * bfhi(z)), 0xfffffffeu);
            }
            st16(rs, WS_W + ((tid >> 3) * 224 + pair) * 128 + (tid & 7) * 16, o);
        }
        STAMP(8);
    } else {
#pragma unroll
        for (int k = 0; k < 13; ++k) {
            const int base = (k * 8 + w) * 64;
            if (base < 6272) {
                const int ch = base + lane, mat = ch >= 3136, cc = ch - mat * 3136, row = cc / 7, kk = cc - row * 7;
                __builtin_amdgcn_global_load_lds((const unsigned*)((mat ? w_up : w_gate) + (size_t)row * F_ + fb * 56 + kk * 8),
                                                 (LAS unsigned*)(smem + SM_KG + base * 16), 16, 0, 0);
            }
        }
        if (fb < 28) {
            float* red = (float*)(smem + SM_M2);
            if (tid < 384) {
                const int rg = tid / 6, ck = tid - rg * 6;
                u32x4 wv[7]; float sv[7];
#pragma unroll
                for (int k = 0; k < 7; ++k) {
                    wv[k] = *(const u32x4*)(w_mod + (size_t)(rg + 64 * k) * (6 * D_) + 3 * D_ + fb * 48 + ck * 8);
                    sv[k] = bf2f(cvec[rg + 64 * k]);
                }
                float acc[8] = {0, 0, 0, 0, 0, 0, 0, 0};
#pragma unroll
                for (int k = 0; k < 7; ++k) {
                    const float s = bfr(sv[k] * frcp(1.f + fexp(-sv[k])));
#pragma unroll
                    for (int j = 0; j < 4; ++j) {
                        acc[2 * j] = __builtin_fmaf(s, bflo(wv[k][j]), acc[2 * j]);
                        acc[2 * j + 1] = __builtin_fmaf(s, bfhi(wv[k][j]), acc[2 * j + 1]);
                    }
                }
#pragma unroll
                for (int j = 0; j < 8; ++j) red[rg * 49 + ck * 8 + j] = acc[j];
            }
            bar();
            float s = 0.f;
            if (tid < 384) {
                const int part = tid / 48, col = tid - part * 48;
#pragma unroll
                for (int k = 0; k < 8; ++k) s += red[(part * 8 + k) * 49 + col];
            }
            bar();
            if (tid < 384) red[tid] = s;
            bar();
            if (tid < 24) {
                float s0 = 0.f, s1 = 0.f;
#pragma unroll
                for (int k = 0; k < 8; ++k) { s0 += red[k * 48 + 2 * tid]; s1 += red[k * 48 + 2 * tid + 1]; }
                st8(rs, WS_MOD2 + (fb * 24 + tid) * 8, (u32x2){pk_bf16(s0, s1), tag});
            }
        }
        STAMP(3);
        asm volatile("s_waitcnt vmcnt(0)" ::: "memory");
        bar();
        const int wq = w < 7 ? w : 0;
        {
            const unsigned char* p = smem + SM_KG + ((fr >> 1) & 1) * 50176 + (8 * fq + (fr >> 2)) * 112 + (8 * wq + 4 * (fr & 1)) * 2;
#pragma unroll
            for (int ks = 0; ks < 14; ++ks) {
                const u32x2 a = tr_read(p + ks * 32 * 112), c2 = tr_read(p + ks * 32 * 112 + 4 * 112);
                fgu[ks] = (u32x4){a[0], a[1], c2[0], c2[1]};
            }
        }
        bar();
#pragma unroll
        for (int k = 0; k < 7; ++k) {
            const int base = (k * 8 + w) * 64;
            if (base < 3136)
                __builtin_amdgcn_global_load_lds((const unsigned*)(w_down + (size_t)fb * 56 * D_ + (base + lane) * 8),
                                                 (LAS unsigned*)(smem + SM_KG + base * 16), 16, 0, 0);
        }
        {
            u32x4* z = (u32x4*)(smem + SM_M2);
            z[tid] = (u32x4){0, 0, 0, 0};
            if (tid < 112) z[512 + tid] = (u32x4){0, 0, 0, 0};
        }
        asm volatile("s_waitcnt vmcnt(0)" ::: "memory");
        bar();
#pragma unroll
        for (int ks = 0; ks < 2; ++ks)
#pragma unroll
            for (int i = 0; i < 4; ++i) {
                int r0 = 32 * ks + 8 * fq + (fr >> 2), r1 = r0 + 4;
                r0 = r0 > 55 ? 55 : r0; r1 = r1 > 55 ? 55 : r1;
                const unsigned char* p = smem + SM_KG + (16 * (4 * wq + i) + 4 * (fr & 3)) * 2;
                const u32x2 a = tr_read(p + r0 * 896), c2 = tr_read(p + r1 * 896);
                fdn[ks * 4 + i] = (u32x4){a[0], a[1], c2[0], c2[1]};
            }
        STAMP(8);
    }

    if (tid < 384) {
        const int i = tid < 336 ? tid : 0;
        u32x4 v;
        for (unsigned spins = 0; spins < SPIN_CAP; ++spins) {
            cfence();
            v = ld16(rs, WS_MOD2 + i * 16);
            if (wave_all(((v[1] ^ tag) | (v[3] ^ tag)) == 0)) break;
            __builtin_amdgcn_s_sleep(2);
        }
        f32x4 o = {bflo(v[0]), bfhi(v[0]), bflo(v[2]), bfhi(v[2])};
        if (i >= 112 && i < 224) {
            const unsigned a = pk_bf16(1.f + o[0], 1.f + o[1]), c2 = pk_bf16(1.f + o[2], 1.f + o[3]);
            o = (f32x4){bflo(a), bfhi(a), bflo(c2), bfhi(c2)};
        }
        if (tid < 336) *(f32x4*)(smem + SM_PRM + i * 16) = o;
    }
    bar();
    float sbf[8], ocf[8], gbf[8];
    {
        const f32x4* pp = (const f32x4*)(smem + SM_PRM) + lo * 2;
#pragma unroll
        for (int h = 0; h < 2; ++h) {
            const f32x4 a = pp[h], c2 = pp[112 + h], d2 = pp[224 + h];
#pragma unroll
            for (int j = 0; j < 4; ++j) { sbf[4 * h + j] = a[j]; ocf[4 * h + j] = c2[j]; gbf[4 * h + j] = d2[j]; }
        }
    }
    STAMP(9);
    if (w < 7) {
        u32x4 v;
        const unsigned off = WS_W + ((g >> 2) * 224 + (tid >> 1)) * 128 + (g & 3) * 32 + (tid & 1) * 16;
        for (unsigned spins = 0; spins < SPIN_CAP; ++spins) {
            cfence();
            v = ld16(rs, off);
            if (wave_all(valid4(v))) break;
            __builtin_amdgcn_s_sleep(2);
        }
        unsigned* tile = (unsigned*)(smem + SM_TILE) + (tid & 1) * 4 * 228 + (tid >> 1);
#pragma unroll
        for (int j = 0; j < 4; ++j) tile[j * 228] = v[j];
    }
    STAMP(10);
    bar();
    {
        const u32x4 wv = *(const u32x4*)(smem + SM_TILE + (w * 228 + lo * 4) * 4);
        float x1[8], h2[8];
#pragma unroll
        for (int j = 0; j < 4; ++j) {
            const unsigned a = pk_bf16(bflo(xv[j]) + bflo(wv[j]), bfhi(xv[j]) + bfhi(wv[j]));
            x1[2 * j] = bflo(a); x1[2 * j + 1] = bfhi(a);
        }
        float mb, rinv;
        ln_stats(x1, act, mb, rinv);
        u32x4 hp;
        float r = 0.f;
#pragma unroll
        for (int j = 0; j < 4; ++j) {
            const unsigned dd = pk_bf16(x1[2 * j] - mb, x1[2 * j + 1] - mb);
            const unsigned xn = pk_bf16(bflo(dd) * rinv, bfhi(dd) * rinv);
            const unsigned a = pk_bf16(bflo(xn) * ocf[2 * j], bfhi(xn) * ocf[2 * j + 1]);
            hp[j] = pk_bf16(bflo(a) + sbf[2 * j], bfhi(a) + sbf[2 * j + 1]);
            h2[2 * j] = bflo(hp[j]); h2[2 * j + 1] = bfhi(hp[j]);
            r = __builtin_fmaf(h2[2 * j], bflo(wrv[j]), r);
            r = __builtin_fmaf(h2[2 * j + 1], bfhi(wrv[j]), r);
        }
        r = wave_sum(act ? r : 0.f);
        const unsigned rb = pk_bf16(r, r) & 0xffffu;
        const unsigned mono = (rb & 0x8000u) ? (~rb & 0xffffu) : (rb | 0x8000u);
        if (lane == 0) st2(rs, WS_R + (w * 256 + g) * 2, umax_(mono, 1u));
        STAMP(11);
        cfence();
        u32x4 o;
#pragma unroll
        for (int j = 0; j < 4; ++j) {
            const unsigned a = pk_bf16(gbf[2 * j] * h2[2 * j], gbf[2 * j + 1] * h2[2 * j + 1]);
            o[j] = pk_bf16(x1[2 * j] + bflo(a), x1[2 * j + 1] + bfhi(a));
            hp[j] = umin_(hp[j], 0xfffffffeu);
        }
        if (act) {
            st16(rs, WS_H2 + t * 896 + lane * 16, hp);
            st16(ro, t * 896 + lane * 16, o);
        }
        if (is_conv) {
            asm volatile("s_waitcnt vmcnt(0)" ::: "memory");
            if (lane == 0) st4(rs, WS_DONE + t * 4, 1u);
            STAMP(12);
            return;
        }
    }

    unsigned* list = (unsigned*)(smem + SM_M2);
    unsigned* sctl = (unsigned*)(smem + SM_M2 + 8192);
    unsigned char* atile = smem + SM_M2 + 8192 + 512;
    unsigned k4[4];
    {
        u32x2 rv;
        bool first = true;
        for (unsigned spins = 0; spins < SPIN_CAP; ++spins) {
            cfence();
            rv = ld8(rs, WS_R + w * 512 + lane * 8);
            const bool ok = (rv[0] & 0xffffu) && (rv[0] >> 16) && (rv[1] & 0xffffu) && (rv[1] >> 16);
            if (first) {
                asm volatile("s_waitcnt vmcnt(0)" ::: "memory");
                if (lane == 0) st4(rs, WS_DONE + t * 4, 1u);
                first = false;
            }
            if (wave_all(ok)) break;
            __builtin_amdgcn_s_sleep(1);
        }
        const unsigned tb = 2047u - (unsigned)(32 * lane + w);
        k4[0] = (rv[0] << 16) | tb; k4[1] = (rv[0] & 0xffff0000u) | (tb - 8u);
        k4[2] = (rv[1] << 16) | (tb - 16u); k4[3] = (rv[1] & 0xffff0000u) | (tb - 24u);
    }
    STAMP(13);
    {
        const unsigned m = wave_umax(umax_(umax_(k4[0], k4[1]), umax_(k4[2], k4[3])));
        if (lane == 0) sctl[8 + w] = m;
    }
    bar();
    {
        const u32x4 a = *(const u32x4*)(sctl + 8), c2 = *(const u32x4*)(sctl + 12);
        const unsigned tau = umin_(umin_(umin_(a[0], a[1]), umin_(a[2], a[3])), umin_(umin_(c2[0], c2[1]), umin_(c2[2], c2[3])));
#pragma unroll
        for (int j = 0; j < 4; ++j)
            if (k4[j] >= tau) list[__hip_atomic_fetch_add(sctl, 1u, __ATOMIC_RELAXED, __HIP_MEMORY_SCOPE_WORKGROUP)] = k4[j];
    }
    bar();
    {
        const int n = (int)sctl[0];
        unsigned* mysel = sctl + 16 + 8 * w;
        for (int base = 0; base < n; base += 64) {
            const unsigned k = list[base + lane];
            unsigned rank = 0;
            for (int i = 0; i < n; i += 4) {
                const u32x4 v = *(const u32x4*)(list + i);
                rank += (v[0] > k) + (v[1] > k) + (v[2] > k) + (v[3] > k);
            }
            if (k != 0 && rank < 8) mysel[rank] = k;
        }
        cfence();
    }
    STAMP(14);
    const unsigned keyA = sctl[16 + 8 * w + (lane & 7)];
    const int tokF = 4 * (lane & 1) + (lane & 2) + ((lane >> 4) & 1);
    const unsigned keyF = sctl[16 + 8 * w + tokF];
    const int tA = 2047 - (int)(keyA & 0xffffu), tF = 2047 - (int)(keyF & 0xffffu);
    const int pc = 7 * fb + (w < 7 ? w : 0);
    unsigned fx = 0, fw = 0, fh = 0;
    if (w < 7) {
        f32x4 acc;
        fx = *(const unsigned*)(x + (size_t)tF * D_ + 2 * pc);
        fw = ld4(rs, WS_W + ((tF >> 5) * 224 + pc) * 128 + (tF & 31) * 4);
        for (unsigned spins = 0; spins < SPIN_CAP; ++spins) {
            cfence();
            u32x4 av[14];
#pragma unroll
            for (int ks = 0; ks < 14; ++ks) av[ks] = ld16(rs, WS_H2 + tA * 896 + ks * 64 + fq * 16);
            fh = ld4(rs, WS_H2 + tF * 896 + pc * 4);
            acc = (f32x4){0, 0, 0, 0};
            unsigned mx = fh;
#pragma unroll
            for (int ks = 0; ks < 14; ++ks) {
                acc = MFMAB(__builtin_bit_cast(bf16x8, av[ks]), __builtin_bit_cast(bf16x8, fgu[ks]), acc);
                mx = umax_(umax_(mx, av[ks][0]), umax_(av[ks][1], umax_(av[ks][2], av[ks][3])));
            }
            if (wave_all(mx != 0xffffffffu)) break;
        }
        STAMP(15);
        const int idx = 2 * (fr >> 3) + (fq >> 1);
        const float lo2 = (idx & 1) ? acc[1] : acc[0], hi2 = (idx & 1) ? acc[3] : acc[2];
        const float keep = (idx & 2) ? hi2 : lo2, send = (idx & 2) ? lo2 : hi2;
        const float recv = dpp_f<0x128>(send);
        const float gg = bfr(fr < 8 ? keep : recv), uu = bfr(fr < 8 ? recv : keep);
        const float sg = bfr(gg * frcp(1.f + fexp(-gg)));
        *(u16*)(atile + (4 * (fq & 1) + idx) * 144 + (8 * w + (fr & 7)) * 2) = (u16)pk_bf16(sg * uu, 0.f);
    }
    bar();
    if (w < 7) {
        const bf16x8 a0 = *(const bf16x8*)(atile + (lane & 7) * 144 + fq * 16);
        const bf16x8 a1 = *(const bf16x8*)(atile + (lane & 7) * 144 + 64 + fq * 16);
#pragma unroll
        for (int i = 0; i < 4; ++i) {
            f32x4 acc = {0, 0, 0, 0};
            acc = MFMAB(a0, __builtin_bit_cast(bf16x8, fdn[i]), acc);
            acc = MFMAB(a1, __builtin_bit_cast(bf16x8, fdn[4 + i]), acc);
            u32x4 o;
#pragma unroll
            for (int j = 0; j < 4; ++j) o[j] = umin_(f2u(acc[j]), 0xfffffffeu);
            if (fq < 2) st16(rs, WS_PART + ((16 * (4 * w + i) + fr) * 32 + fb) * 32 + fq * 16, o);
        }
        STAMP(16);
        u32x4 c0, c1;
        for (unsigned spins = 0; spins < SPIN_CAP; ++spins) {
            cfence();
            c0 = ld16(rs, WS_PART + (2 * pc) * 1024 + lane * 16);
            c1 = ld16(rs, WS_PART + (2 * pc + 1) * 1024 + lane * 16);
            const unsigned dn = ld4(rs, WS_DONE + tF * 4);
            if (wave_all(valid4(c0) && valid4(c1) && dn == 1u)) break;
            __builtin_amdgcn_s_sleep(1);
        }
        STAMP(17);
        float v[4];
#pragma unroll
        for (int j = 0; j < 4; ++j) v[j] = fold32(u2f(c0[j]), u2f(c1[j]));
        float u0 = fold16(v[0], v[1]), u1 = fold16(v[2], v[3]);
        u0 += dpp_f<0x128>(u0); u1 += dpp_f<0x128>(u1);
        u0 += dpp_f<0x124>(u0); u1 += dpp_f<0x124>(u1);
        u0 += dpp_f<0x4E>(u0);  u1 += dpp_f<0x4E>(u1);
        const float s = (lane & 2) ? u1 : u0;
        const int hi = lane >> 5;
        const unsigned mono = keyF >> 16;
        const unsigned rb = (mono & 0x8000u) ? (mono & 0x7fffu) : (~mono & 0xffffu);
        const float rsel = bfr(frcp(1.f + fexp(-u2f(rb << 16))));
        const float x1 = bfr((hi ? bfhi(fx) : bflo(fx)) + (hi ? bfhi(fw) : bflo(fw)));
        const float y2 = bfr(bfr(s) * rsel);
        const float bb = bfr((hi ? bfhi(fh) : bflo(fh)) + y2);
        const float gbv = ((const float*)(smem + SM_PRM))[896 + 2 * pc + hi];
        const float o = x1 + bfr(gbv * bb);
        if ((lane & 12) == 0) st2(ro, tF * 896 + (2 * pc + hi) * 2, pk_bf16(o, o) & 0xffffu);
        STAMP(18);
    }
}

extern "C" void kernel_launch(void* const* d_in, const int* in_sizes, int n_in,
                              void* d_out, int out_size, void* d_ws, size_t ws_size,
                              hipStream_t stream) {
    fused_block<<<256, 512, 0, stream>>>(
        (const u16*)d_in[0], (const u16*)d_in[1], (const u16*)d_in[2], (const u16*)d_in[3], (const u16*)d_in[4],
        (const u16*)d_in[5], (const u16*)d_in[6], (const u16*)d_in[7], (const u16*)d_in[8], (const u16*)d_in[10],
        (const u16*)d_in[11], (const u16*)d_in[12], (const u16*)d_in[13], (const u16*)d_in[14], (const u16*)d_in[15],
        (unsigned char*)d_ws, (u16*)d_out);
}
```

```cpp
#include <hip/hip_runtime.h>
#include <stdint.h>

typedef _Float16 half8 __attribute__((ext_vector_type(8)));
typedef _Float16 half2v __attribute__((ext_vector_type(2)));
typedef __bf16 bf16x8 __attribute__((ext_vector_type(8)));
typedef __bf16 bf16x2 __attribute__((ext_vector_type(2)));
typedef float f32x4 __attribute__((ext_vector_type(4)));
typedef unsigned u32x4 __attribute__((ext_vector_type(4)));
typedef unsigned u32x2 __attribute__((ext_vector_type(2)));
typedef unsigned short u16;
#if defined(__HIP_DEVICE_COMPILE__)
#define NOPK __attribute__((target("no-packed-fp32-ops")))
#else
#define NOPK
#endif
#define DEV NOPK __device__ __forceinline__

constexpr int L_ = 2048, D_ = 448, N_ = 256, F_ = 1792;

constexpr unsigned WS_E    = 0;
constexpr unsigned WS_XG   = 4096;
constexpr unsigned WS_W    = WS_XG + 224u * 2048u * 8u;
constexpr unsigned WS_H2   = WS_W + L_ * D_ * 2;
constexpr unsigned WS_PART = WS_H2 + L_ * D_ * 2;
constexpr unsigned WS_R    = WS_PART + 448u * 32u * 32u;
constexpr unsigned WS_SEL  = WS_R + 4096;
constexpr unsigned WS_MOD2 = WS_SEL + 8192;
constexpr unsigned WS_MP   = WS_MOD2 + 5376 + 2816;
constexpr unsigned WS_END  = WS_MP + 2688u * 64u;

#define STAMP_DECL
#define STAMP(i)
#define STAMP_FLUSH

DEV float u2f(unsigned u) { return __builtin_bit_cast(float, u); }
DEV unsigned f2u(float f) { return __builtin_bit_cast(unsigned, f); }
DEV void bar() { asm volatile("s_waitcnt lgkmcnt(0)\n\ts_barrier" ::: "memory"); }
DEV void cfence() { asm volatile("" ::: "memory"); }
DEV bool wave_all(bool p) { return __builtin_amdgcn_ballot_w64(!p) == 0; }
DEV float bf2f(u16 v) { return u2f((unsigned)v << 16); }
DEV float bflo(unsigned v) { return u2f(v << 16); }
DEV float bfhi(unsigned v) { return u2f(v & 0xffff0000u); }
typedef float f32x2 __attribute__((ext_vector_type(2)));
DEV unsigned pk_bf16(float a, float b) {
    const f32x2 v = {a, b};
    unsigned r = __builtin_bit_cast(unsigned, __builtin_convertvector(v, bf16x2));
    asm("" : "+v"(r));
    return r;
}
DEV float bfr(float x) { return bfhi(pk_bf16(x, x)); }
DEV unsigned pk_f16(float a, float b) {
    asm("" : "+v"(a), "+v"(b));
    const f32x2 v = {a, b};
    return __builtin_bit_cast(unsigned, __builtin_convertvector(v, half2v));
}
DEV unsigned pk_f16n(float a, float b) {
    asm("" : "+v"(a), "+v"(b));
    const f32x2 v = {a, -b};
    return __builtin_bit_cast(unsigned, __builtin_convertvector(v, half2v));
}
DEV unsigned pk_f16m(float a, float b) {
    const f32x2 v = {a, b};
    return __builtin_bit_cast(unsigned, __builtin_convertvector(v, half2v));
}
DEV float dot2bf(unsigned a, unsigned b, float c) {
    return __builtin_amdgcn_fdot2_f32_bf16(__builtin_bit_cast(bf16x2, a), __builtin_bit_cast(bf16x2, b), c, false);
}
DEV unsigned umax_(unsigned a, unsigned b) { return a > b ? a : b; }
DEV unsigned umin_(unsigned a, unsigned b) { return a < b ? a : b; }
template <int CTRL> DEV unsigned dpp_u(unsigned v) {
    return (unsigned)__builtin_amdgcn_update_dpp(0, (int)v, CTRL, 0xf, 0xf, true);
}
template <int CTRL> DEV float dpp_f(float v) { return u2f(dpp_u<CTRL>(f2u(v))); }
DEV float row_sum(float v) {
    v += dpp_f<0xB1>(v);
    v += dpp_f<0x4E>(v);
    v += dpp_f<0x141>(v);
    v += dpp_f<0x140>(v);
    return v;
}
DEV float fold32(float a, float b) {
    auto r = __builtin_amdgcn_permlane32_swap(f2u(a), f2u(b), false, false);
    return u2f(r[0]) + u2f(r[1]);
}
DEV float fold16(float a, float b) {
    auto r = __builtin_amdgcn_permlane16_swap(f2u(a), f2u(b), false, false);
    return u2f(r[0]) + u2f(r[1]);
}
DEV float rdlane(float v, int l) { return u2f((unsigned)__builtin_amdgcn_readlane((int)f2u(v), l)); }
DEV void wave_sum2(float a, float b, float& sa, float& sb) {
    float v = fold32(a, b);
    v = fold16(v, v);
    v = row_sum(v);
    sa = rdlane(v, 0); sb = rdlane(v, 32);
}
DEV float wave_sum(float a) {
    float v = fold32(a, a);
    v = fold16(v, v);
    return row_sum(v);
}
DEV unsigned wave_umax(unsigned v) {
    v = umax_(v, dpp_u<0xB1>(v));
    v = umax_(v, dpp_u<0x4E>(v));
    v = umax_(v, dpp_u<0x141>(v));
    v = umax_(v, dpp_u<0x140>(v));
    { auto r = __builtin_amdgcn_permlane16_swap(v, v, false, false); v = umax_(r[0], r[1]); }
    { auto r = __builtin_amdgcn_permlane32_swap(v, v, false, false); v = umax_(r[0], r[1]); }
    return v;
}
DEV int exp_above(float m) {
    int e = (int)((f2u(m) >> 23) & 0xff) - 126;
    return e < -100 ? -100 : (e > 100 ? 100 : e);
}
DEV float pow2i(int e) { return u2f((unsigned)(e + 127) << 23); }
DEV float fexp(float x) { return __builtin_amdgcn_exp2f(x * 1.44269504088896341f); }
DEV float frcp(float x) { return __builtin_amdgcn_rcpf(x); }
DEV bool valid4(u32x4 v) { return umax_(umax_(v[0], v[1]), umax_(v[2], v[3])) != 0xffffffffu; }

typedef __amdgpu_buffer_rsrc_t rsrc_t;
DEV u32x4 ld16(rsrc_t r, unsigned off) { return __builtin_amdgcn_raw_buffer_load_b128(r, off, 0, 16); }
DEV u32x2 ld8(rsrc_t r, unsigned off) { return __builtin_amdgcn_raw_buffer_load_b64(r, off, 0, 16); }
DEV unsigned ld4(rsrc_t r, unsigned off) { return __builtin_amdgcn_raw_buffer_load_b32(r, off, 0, 16); }
DEV void st16(rsrc_t r, unsigned off, u32x4 v) { __builtin_amdgcn_raw_buffer_store_b128(v, r, off, 0, 16); }
DEV void st8(rsrc_t r, unsigned off, u32x2 v) { __builtin_amdgcn_raw_buffer_store_b64(v, r, off, 0, 16); }
DEV void st4(rsrc_t r, unsigned off, unsigned v) { __builtin_amdgcn_raw_buffer_store_b32(v, r, off, 0, 16); }
DEV void st2(rsrc_t r, unsigned off, unsigned v) { __builtin_amdgcn_raw_buffer_store_b16((u16)v, r, off, 0, 16); }
constexpr unsigned SPIN_CAP = 1u << 15;
#define OPAQUE(v) asm volatile("" : "+v"(v) :: "memory")
#define PIN(v) asm volatile("" : "+v"(v))

constexpr int IMG_R1 = 0, IMG_RX = 560, IMG_RC = 608, IMG_RD = 864;
constexpr int lq_of(int l) {
    if (l < 256) return l;
    int s = l < 512 ? 1 : (l < 1024 ? 2 : 3);
    int q = l >> s, rem = l & ((1 << s) - 1), half = 1 << (s - 1);
    if (rem > half || (rem == half && (q & 1))) q++;
    return q << s;
}
constexpr int slot_of(int c) {
    if (c < 256) return (c & 15) * 16 + (c >> 4);
    if (c < 512) return (16 + ((c & 15) >> 1)) * 16 + ((c - 256) >> 4);
    if (c < 1024) return (24 + 4 * ((c - 512) >> 8) + ((c & 15) >> 2)) * 16 + ((c & 255) >> 4);
    if (c < 2048) return (32 + 2 * ((c - 1024) >> 8) + ((c & 15) >> 3)) * 16 + ((c & 255) >> 4);
    return 40 * 16;
}
struct KTab { u16 v[1032]; };
constexpr KTab make_ktab() {
    KTab t{};
    for (int n = 0; n < 1032; ++n) t.v[n] = 2 * 768;
    for (int n = 0; n < 560; ++n) { int l = 543 - n; if (l >= 0 && l <= 514) t.v[IMG_R1 + n] = (u16)(2 * slot_of(lq_of(l))); }
    for (int n = 0; n < 48; ++n) { int l = 1055 - n; if (l == 1027 || l == 1028) t.v[IMG_RX + n] = (u16)(2 * slot_of(1024)); }
    const int coffC[4] = {16, 4, 8, 12};
    for (int e = 0; e < 4; ++e)
        for (int n = 0; n < 64; ++n) { int lam = 79 - n; if (lam >= 32 && lam <= 63) t.v[IMG_RC + e * 64 + n] = (u16)(2 * slot_of(16 * lam + coffC[e])); }
    const int coffD[2] = {16, 8};
    for (int e = 0; e < 2; ++e)
        for (int n = 0; n < 80; ++n) { int lam = 127 - n; if (lam >= 64 && lam <= 127) t.v[IMG_RD + e * 80 + n] = (u16)(2 * slot_of(16 * lam + coffD[e])); }
    return t;
}
__device__ __attribute__((aligned(16))) const KTab g_ktab = make_ktab();

struct cf { float r, i; };
DEV cf cmul(cf a, cf b) { return {a.r * b.r - a.i * b.i, a.r * b.i + a.i * b.r}; }
DEV cf csqr(cf a) { return {a.r * a.r - a.i * a.i, (a.r + a.r) * a.i}; }
DEV cf cexp_pow2(float t2, float rh, float rl, float Lf) {
    float mag = __builtin_amdgcn_exp2f(t2 * Lf);
    float a = rh * Lf;
    float ph = (a - __builtin_rintf(a)) + rl * Lf;
    return {mag * __builtin_amdgcn_cosf(ph), mag * __builtin_amdgcn_sinf(ph)};
}
#define MFMA16(a, b, c) __builtin_amdgcn_mfma_f32_16x16x32_f16(a, b, c, 0, 0, 0)
#define MFMAB(a, b, c) __builtin_amdgcn_mfma_f32_16x16x32_bf16(a, b, c, 0, 0, 0)

DEV void ln_stats(const float (&f)[8], bool act, float& mb, float& rinv) {
    const float pv = u2f((unsigned)__builtin_amdgcn_readfirstlane((int)f2u(f[0])));
    float s1 = 0.f, s2 = 0.f;
#pragma unroll
    for (int j = 0; j < 8; ++j) { const float dd = f[j] - pv; s1 += dd; s2 = __builtin_fmaf(dd, dd, s2); }
    s1 = act ? s1 : 0.f; s2 = act ? s2 : 0.f;
    float t1, t2;
    wave_sum2(s1, s2, t1, t2);
    const float dm = t1 * (1.f / D_);
    const float var = __builtin_fmaxf(__builtin_fmaf(-dm, dm, t2 * (1.f / D_)), 0.f);
    mb = bfr(pv + dm);
    rinv = frcp(bfr(__builtin_amdgcn_sqrtf(bfr(bfr(var) + 1e-5f))));
}

constexpr int KG_ROW = 288, KG_BUF = 16 * KG_ROW, KG_WAVE = 2 * KG_BUF;
constexpr int KC_STR = 2112;
constexpr int U_PAD = 256, U_LEN = U_PAD + L_;
constexpr int SM_KG = 0;
constexpr int SM_T16 = 73728;
constexpr int SM_KC = 76928;
constexpr int SM_U = 93824;
constexpr int SM_TILE = 103040;
constexpr int SM_PRM = 110336;
constexpr int SM_MISC = 115712;
constexpr int SM_MR = 116736;
constexpr int SM_SEL = SM_MR + 3 * 2048;
constexpr int SM_TOTAL = SM_SEL + 2048;
#define LAS __attribute__((address_space(3)))
typedef short s16x4 __attribute__((ext_vector_type(4)));
DEV u32x2 tr_read(const unsigned char* p) {
    return __builtin_bit_cast(u32x2, __builtin_amdgcn_ds_read_tr16_b64_v4i16((LAS s16x4*)p));
}
DEV half8 kfrag(const unsigned char* pa, const unsigned char* pb) {
    const u32x2 a = *(const volatile LAS u32x2*)pa, b = *(const volatile LAS u32x2*)pb;
    const u32x4 v = {a[0], a[1], b[0], b[1]};
    return __builtin_bit_cast(half8, v);
}
DEV half8 box_frag(int p, int koff, int phif, int wd) {
    int sh = p - phif - koff - (wd - 1);
    sh = sh > 31 ? 31 : sh;
    const unsigned m = (unsigned)(((unsigned long long)((1u << wd) - 1u) << (sh + 32)) >> 32);
    u32x4 v;
#pragma unroll
    for (int k = 0; k < 4; ++k) {
        const unsigned x = (m >> (2 * k)) & 3u;
        v[k] = ((x | (x << 15)) & 0x10001u) * 0x3C00u;
    }
    return __builtin_bit_cast(half8, v);
}
DEV half8 pack_v(f32x4 a, f32x4 b) {
    u32x4 v = {pk_f16m(a[0], a[1]), pk_f16m(a[2], a[3]), pk_f16m(b[0], b[1]), pk_f16m(b[2], b[3])};
    return __builtin_bit_cast(half8, v);
}
template <int OFF> DEV void st4rows(unsigned base, unsigned a, unsigned b, unsigned c, unsigned d) {
    asm volatile("s_mov_b32 m0, %4\n\ts_nop 0\n\tds_write_addtid_b32 %0 offset:%5\n\tds_write_addtid_b32 %1 offset:%6\n\t"
                 "ds_write_addtid_b32 %2 offset:%7\n\tds_write_addtid_b32 %3 offset:%8"
                 :: "v"(a), "v"(b), "v"(c), "v"(d), "s"(base), "i"(OFF), "i"(OFF + KG_ROW), "i"(OFF + 2 * KG_ROW), "i"(OFF + 3 * KG_ROW) : "memory");
}
DEV unsigned pkc(cf v) { unsigned r; asm("v_cvt_pk_f16_f32 %0, %1, %2" : "=v"(r) : "v"(v.r), "v"(v.i)); return r; }
DEV unsigned pkj(cf v) { unsigned r; asm("v_cvt_pk_f16_f32 %0, %1, -%2" : "=v"(r) : "v"(v.r), "v"(v.i)); return r; }

struct KF { half8 a[4], b[4], c[3], x[4], bx[4]; };

template <int HS>
DEV void conv_kfrags(KF& k, const unsigned char* kb1, const unsigned char* kb2, int fr, int koffb) {
#pragma unroll
    for (int rr = 0; rr < 4; ++rr) {
        const int r = 4 * HS + rr;
        k.a[rr] = kfrag(kb1 + 64 * (16 - r), kb1 + 64 * (16 - r) + 8);
        k.b[rr] = kfrag(kb1 + 64 * (8 - r), kb1 + 64 * (8 - r) + 8);
    }
    constexpr int ea = HS ? 0 : 1, eb = HS ? 3 : 2;
#pragma unroll
    for (int s = 2; s <= 4; ++s)
        k.c[s - 2] = kfrag(kb2 + 2 * (IMG_RC + ea * 64 + 16 * (4 - s)), kb2 + 2 * (IMG_RC + eb * 64 + 16 * (4 - s)));
    if (HS == 0) {
        k.x[0] = kfrag(kb1, kb1 + 8);
        k.x[1] = kfrag(kb1 + 2 * IMG_RX, kb1 + 2 * IMG_RX + 8);
        k.x[2] = k.x[0]; k.x[3] = k.x[0];
    } else {
#pragma unroll
        for (int s = 4; s <= 7; ++s)
            k.x[s - 4] = kfrag(kb2 + 2 * (IMG_RD + 16 * (7 - s)), kb2 + 2 * (IMG_RD + 80 + 16 * (7 - s)));
    }
}

template <int HS, int TG>
DEV void conv_mfma(const unsigned char* Uc, const KF& k, f32x4* red) {
    constexpr int I0 = TG ? 5 : 0, I1 = TG ? 7 : 4, NT = I1 - I0 + 1;
    constexpr int C0 = TG ? 1 : 0, C1 = TG ? 5 : 2;
    constexpr int D1 = TG ? 3 : 0;
    const f32x4 zero4 = {0, 0, 0, 0};
    f32x4 acc[NT];
#pragma unroll
    for (int i = 0; i < NT; ++i) acc[i] = zero4;
    half8 fc[6], fd[4];
#define UFRAG(delta) (*(const half8*)(Uc + 64 * (delta)))
#define ACC(I, a, b) do { if ((I) >= I0 && (I) <= I1) acc[(I) - I0] = MFMA16(a, b, acc[(I) - I0]); } while (0)
    if (HS == 0) {
        constexpr int M0 = TG ? 1 : 0, M1 = I1;
        half8 ub[8];
#pragma unroll
        for (int m = M0; m <= M1; ++m) ub[m] = UFRAG(8 * m);
#pragma unroll
        for (int m = C0; m <= C1; ++m) fc[m] = pack_v(MFMA16(ub[m], k.bx[0], zero4), MFMA16(ub[m], k.bx[1], zero4));
#pragma unroll
        for (int m = M0; m <= M1; ++m) {
            ACC(m, k.a[0], ub[m]); ACC(m + 1, k.b[0], ub[m]); ACC(m + 2, k.x[0], ub[m]); ACC(m + 4, k.x[1], ub[m]);
        }
    } else {
        half8 ua[6], ub[4];
#pragma unroll
        for (int m = 0; m <= C1; ++m) ua[m] = *(const half8*)(Uc + 64 * 8 * m - 16);
#pragma unroll
        for (int m = 0; m <= D1; ++m) ub[m] = UFRAG(8 * m);
#pragma unroll
        for (int m = C0; m <= C1; ++m) fc[m] = pack_v(MFMA16(ua[m], k.bx[0], zero4), MFMA16(ua[m], k.bx[1], zero4));
#pragma unroll
        for (int m = 0; m <= D1; ++m) fd[m] = pack_v(MFMA16(ua[m], k.bx[2], zero4), MFMA16(ub[m], k.bx[3], zero4));
    }
    constexpr int MM0 = TG ? 4 : 0;
#pragma unroll
    for (int rr = (HS == 0 ? 1 : 0); rr < 4; ++rr) {
        const int r = 4 * HS + rr;
        half8 ub[8];
#pragma unroll
        for (int m = MM0; m <= I1; ++m) ub[m] = UFRAG(8 * m - r);
#pragma unroll
        for (int m = MM0; m <= I1; ++m) { ACC(m, k.a[rr], ub[m]); ACC(m + 1, k.b[rr], ub[m]); }
    }
#pragma unroll
    for (int s = 2; s <= 4; ++s)
#pragma unroll
        for (int m = C0; m <= C1; ++m) ACC(m + s, fc[m], k.c[s - 2]);
    if (HS == 1) {
#pragma unroll
        for (int s = 4; s <= I1; ++s)
#pragma unroll
            for (int m = 0; m <= D1; ++m) ACC(m + s, fd[m], k.x[s - 4]);
    }
#undef UFRAG
#undef ACC
#pragma unroll
    for (int i = 0; i < NT; ++i) red[64 * (I0 + i)] = acc[i];
}

DEV void mod_unit_pos(int u, int& cg, int& rg) {
    const int v = u < 147 ? u : u - 147;
    rg = v / 21; cg = v - rg * 21 + (u < 147 ? 0 : 21);
}
DEV void mod_unit_a(u32x4 wv, u16 cval, float* red, int lane, int w) {
    const float cvf = bf2f(cval);
    const float s = bfr(cvf * frcp(1.f + fexp(-cvf)));
    float a[4];
#pragma unroll
    for (int j = 0; j < 4; ++j) a[j] = fold32(s * bflo(wv[j]), s * bfhi(wv[j]));
    float b0 = fold16(a[0], a[1]), b1 = fold16(a[2], a[3]);
    b0 += dpp_f<0x128>(b0); b1 += dpp_f<0x128>(b1);
    if ((lane & 8) == 0) {
        const int col = (lane & 7) * 8 + 2 * ((lane >> 4) & 1) + (lane >> 5);
        red[w * 64 + col] = b0; red[w * 64 + col + 4] = b1;
    }
}
DEV void mod_unit_b(const float* red, int cg, int rg, unsigned tag, rsrc_t rs, int lane) {
    float t = 0.f;
#pragma unroll
    for (int k = 0; k < 8; ++k) t += red[k * 64 + lane];
    st8(rs, WS_MP + (cg * 64 + lane) * 64 + rg * 8, (u32x2){f2u(t), tag});
}

#define GAS __attribute__((address_space(1)))
typedef const GAS u16* gptr;
NOPK __global__ __launch_bounds__(512, 2) void fused_block(
    const u16* x_, const u16* A_re_, const u16* A_im_, const u16* B_re_, const u16* B_im_, const u16* C_re_,
    const u16* C_im_, const u16* log_dt_, const u16* Dskip_, const u16* w_r_, const u16* w_gate_, const u16* w_up_,
    const u16* w_down_, const u16* cvec_, const u16* w_mod_, unsigned char* ws, u16* out)
{
    __shared__ __attribute__((aligned(16))) unsigned char smem[SM_TOTAL];
    gptr x = (gptr)x_, A_re = (gptr)A_re_, A_im = (gptr)A_im_, B_re = (gptr)B_re_, B_im = (gptr)B_im_, C_re = (gptr)C_re_;
    gptr C_im = (gptr)C_im_, log_dt = (gptr)log_dt_, Dskip = (gptr)Dskip_, w_r = (gptr)w_r_, w_gate = (gptr)w_gate_;
    gptr w_up = (gptr)w_up_, w_down = (gptr)w_down_, cvec = (gptr)cvec_, w_mod = (gptr)w_mod_;
    asm volatile("" : "+s"(x), "+s"(A_re), "+s"(A_im), "+s"(cvec), "+s"(w_mod), "+s"(w_r)
                 : "s"(B_re), "s"(B_im), "s"(C_re), "s"(C_im), "s"(log_dt), "s"(Dskip), "s"(w_gate), "s"(w_up), "s"(w_down), "s"(out), "s"(ws));
    const int tid = threadIdx.x, lane = tid & 63;
    const int w = __builtin_amdgcn_readfirstlane(tid >> 6);
    const int b = blockIdx.x, xg = b & 7, jj = b >> 3;
    const int g = xg * 32 + jj;
    const bool is_conv = jj < 28;
    const int pair = xg * 28 + jj;
    const int fb = xg * 4 + (jj - 28);
    const int t = 8 * g + w;
    const bool act = lane < 56;
    const int lo = act ? lane : 0;
    const int fr = lane & 15, fq = lane >> 4;
    const rsrc_t rs = __builtin_amdgcn_make_buffer_rsrc(ws, 0, WS_END, 0x00020000);
    const rsrc_t ro = __builtin_amdgcn_make_buffer_rsrc(out, 0, L_ * D_ * 2, 0x00020000);
    float* misc = (float*)(smem + SM_MISC);
    const u32x4 ones = {0xffffffffu, 0xffffffffu, 0xffffffffu, 0xffffffffu};
    STAMP_DECL;
    STAMP(0);

    const u32x4 xv = *(const GAS u32x4*)(x + (size_t)t * D_ + lo * 8);
    __builtin_amdgcn_sched_barrier(0);
    unsigned cwarm;
    {
        unsigned long long pc; unsigned len;
        asm volatile("s_getpc_b64 %0\n.Lcode_mark_a:\n\ts_mov_b32 %1, .Lcode_mark_b-.Lcode_mark_a" : "=s"(pc), "=s"(len));
        unsigned o = (unsigned)(jj * 16 + (lane & 15)) * 128u;
        o = o < len ? o : 0u;
        cwarm = *(const GAS unsigned*)(pc + o);
    }
    const unsigned tag = *(const unsigned*)(ws + WS_E) + 1u;
    cfence();
    int ucg, urg, cg2, rg2, cg3, rg3;
    mod_unit_pos(b, ucg, urg);
    mod_unit_pos(is_conv ? 0 : 256 + fb, cg2, rg2);
    mod_unit_pos(is_conv ? 0 : (fb < 6 ? 288 + fb : 256 + fb), cg3, rg3);
    const unsigned uoff = (unsigned)((tid >> 3) * (6 * D_) + (tid & 7) * 8);
    u32x4 umv = *(const GAS u32x4*)(w_mod + (urg * 64 * (6 * D_) + ucg * 64) + uoff);
    unsigned ucv = cvec[urg * 64 + (tid >> 3)];
    const unsigned pi = is_conv ? (unsigned)(pair * 512 + tid) : (unsigned)(tid >> 3);
    unsigned p_are = (is_conv ? A_re : cvec + rg2 * 64)[pi];
    unsigned p_aim = (is_conv ? A_im : cvec + rg3 * 64)[pi];
    unsigned p_bre = B_re[pi], p_bim = B_im[pi], p_cre = C_re[pi], p_cim = C_im[pi];
    unsigned p_ldt = log_dt[(is_conv ? 2 * pair : 0) + (tid >> 8)];
    typedef u32x4 __attribute__((aligned(8))) u32x4a8;
    const uintptr_t um2b = is_conv ? (uintptr_t)g_ktab.v : (uintptr_t)(w_mod + (rg2 * 64 * (6 * D_) + cg2 * 64));
    u32x4 um2 = *(const __attribute__((address_space(1))) u32x4a8*)(um2b + 2u * (is_conv ? 4u * (tid & 255) : uoff));
    u32x4 um3 = *(const GAS u32x4*)((is_conv ? w_r : w_mod + (rg3 * 64 * (6 * D_) + cg3 * 64)) + (is_conv ? 0u : uoff));
    u32x4 wrv = *(const GAS u32x4*)(w_r + lo * 8);
    cfence();
    if (tid < 448) {
        const unsigned wo = is_conv ? (unsigned)(g * 7168 + tid * 16)
                                    : (unsigned)(((g >> 2) * 224 + (tid >> 1)) * 128 + (g & 3) * 32 + (tid & 1) * 16);
        st16(rs, WS_W + wo, ones);
    }
    cfence();
    const int cty = (w & 3) ^ (w >> 2), hs = cty & 1, tg = cty >> 1;
    const int koffb = -16 * (fq >> 1) + 8 * (fq & 1);
    KF kf;
    kf.bx[0] = box_frag(fr, koffb - 8 * hs, hs ? 14 : 3, hs ? 5 : 3);
    kf.bx[1] = box_frag(fr, koffb - 8 * hs, hs ? 11 : 6, hs ? 3 : 5);
    kf.bx[2] = box_frag(fr, koffb - 8, 12, 9);
    kf.bx[3] = box_frag(fr, koffb, 5, 7);
#pragma unroll
    for (int i = 0; i < 4; ++i) PIN(kf.bx[i]);
    cfence();
    float xf32[8];
    {
#pragma unroll
        for (int j = 0; j < 4; ++j) { xf32[2 * j] = bflo(xv[j]); xf32[2 * j + 1] = bfhi(xv[j]); }
        float mb, rinv;
        ln_stats(xf32, act, mb, rinv);
        u32x4 o;
#pragma unroll
        for (int j = 0; j < 4; ++j) {
            const unsigned dd = pk_bf16(xf32[2 * j] - mb, xf32[2 * j + 1] - mb);
            o[j] = pk_bf16(bflo(dd) * rinv, bfhi(dd) * rinv);
        }
        if (act) *(u32x4*)(smem + SM_TILE + (w * 228 + lane * 4) * 4) = o;
    }
    STAMP(1);
    float* mred = (float*)(smem + SM_MR);
    PIN(umv); PIN(ucv);
    mod_unit_a(umv, (u16)ucv, mred, lane, w);
    asm volatile("s_waitcnt vmcnt(0)" ::: "memory");
    PIN(cwarm); PIN(wrv); PIN(um2); PIN(um3); PIN(p_are); PIN(p_aim); PIN(p_bre); PIN(p_bim); PIN(p_cre); PIN(p_cim); PIN(p_ldt);
    bar();
    if (w < 7) {
        const unsigned* tile = (const unsigned*)(smem + SM_TILE);
#pragma unroll
        for (int it = 0; it < 2; ++it) {
            const int e = tid + 448 * it, pr = e >> 2, k = e & 3;
            const u32x4 v = {tile[(2 * k) * 228 + pr], tag, tile[(2 * k + 1) * 228 + pr], tag};
            st16(rs, WS_XG + pr * 16384 + (8 * g + 2 * k) * 8, v);
        }
    } else {
        mod_unit_b(mred, ucg, urg, tag, rs, lane);
    }
    STAMP(2);
    cfence();

    u32x4 fgu[14], fdn[8];
    u32x4 m2v;
    const unsigned m2off = WS_MOD2 + (tid < 336 ? tid : 0) * 16;
    if (is_conv) {
        const int c = w >> 2;
        const unsigned dsk2 = *(const GAS unsigned*)(Dskip + 2 * pair);
        unsigned warm = 0;
        const u32x2 kix0 = {um2[0], um2[1]}, kix1 = {um2[2], um2[3]};
        const int mpl = lane < 48 ? lane : 0;
        unsigned mpoff = WS_MP + ((mpl >> 4) * D_ + 2 * pair) * 64 + (mpl & 15) * 8;
        u32x2 mpv = {0, 0};
        {
            const float dt = bfr(expf(bflo(p_ldt)));
            float zr = dt * bflo(p_are);
            const float zi = dt * bflo(p_aim);
            const float dbr = dt * bflo(p_bre), dbi = dt * bflo(p_bim);
            const float cre = bflo(p_cre), cim = bflo(p_cim);
            cf cb = {cre * dbr - cim * dbi, cre * dbi + cim * dbr};
            float mx, rho;
            {
                const unsigned a = f2u(__builtin_fmaxf(__builtin_fabsf(cb.r), __builtin_fabsf(cb.i)));
                const unsigned bb = f2u(__builtin_fmaxf(zr, 0.f));
                auto r = __builtin_amdgcn_permlane32_swap(a, bb, false, false);
                unsigned v = umax_(r[0], r[1]);
                { auto q = __builtin_amdgcn_permlane16_swap(v, v, false, false); v = umax_(q[0], q[1]); }
                v = umax_(v, dpp_u<0xB1>(v)); v = umax_(v, dpp_u<0x4E>(v));
                v = umax_(v, dpp_u<0x141>(v)); v = umax_(v, dpp_u<0x140>(v));
                mx = rdlane(u2f(v), 0); rho = rdlane(u2f(v), 32);
            }
            zr -= rho;
            const int ew = exp_above(mx);
            const float sc = pow2i(14 - ew);
            cb.r *= sc; cb.i *= sc;
            const float t2 = zr * 1.44269504088896341f;
            const float rh = zi * 0.15915494309189535f;
            const float rl = __builtin_fmaf(zi, 0.15915494309189535f, -rh) + zi * 6.4206382e-09f;
            unsigned char* wb = smem + SM_KG + w * KG_WAVE;
            const unsigned wba = (unsigned)(size_t)(LAS unsigned char*)wb;
            const unsigned char* rd0 = wb + fr * KG_ROW + fq * 16;
            const unsigned char* rd1 = rd0 + KG_BUF;
            {
                cf s1 = cexp_pow2(t2, rh, rl, 16.f), s2 = csqr(s1), s4 = csqr(s2), s8 = csqr(s4);
                cf q0 = cb, q1 = cmul(q0, s1), q2 = cmul(q0, s2), q3 = cmul(q1, s2);
                st4rows<0>(wba, pkj(q0), pkj(q1), pkj(q2), pkj(q3));
                cf q4 = cmul(q0, s4), q5 = cmul(q1, s4), q6 = cmul(q2, s4), q7 = cmul(q3, s4);
                st4rows<4 * KG_ROW>(wba, pkj(q4), pkj(q5), pkj(q6), pkj(q7));
                st4rows<8 * KG_ROW>(wba, pkj(cmul(q0, s8)), pkj(cmul(q1, s8)), pkj(cmul(q2, s8)), pkj(cmul(q3, s8)));
                st4rows<12 * KG_ROW>(wba, pkj(cmul(q4, s8)), pkj(cmul(q5, s8)), pkj(cmul(q6, s8)), pkj(cmul(q7, s8)));
            }
            cf p1 = cexp_pow2(t2, rh, rl, 1.f), p2 = csqr(p1), p3 = cmul(p2, p1), p4 = csqr(p2);
            st4rows<KG_BUF>(wba, 0x00003C00u, pkc(p1), pkc(p2), pkc(p3));
            cf p5 = cmul(p4, p1), p6 = cmul(p4, p2), p7 = cmul(p4, p3), p8 = csqr(p4);
            st4rows<KG_BUF + 4 * KG_ROW>(wba, pkc(p4), pkc(p5), pkc(p6), pkc(p7));
            cf p10 = cmul(p8, p2), p12 = cmul(p8, p4), p14 = cmul(p8, p6);
            st4rows<KG_BUF + 8 * KG_ROW>(wba, pkc(p8), pkc(cmul(p8, p1)), pkc(p10), pkc(cmul(p8, p3)));
            st4rows<KG_BUF + 12 * KG_ROW>(wba, pkc(p12), pkc(cmul(p8, p5)), pkc(p14), pkc(cmul(p8, p7)));
            half8 xf[4], ff[4];
            f32x4 acc0 = {0, 0, 0, 0}, acc1 = {0, 0, 0, 0}, acc2 = {0, 0, 0, 0};
#pragma unroll
            for (int s = 0; s < 4; ++s) { xf[s] = *(const half8*)(rd0 + s * 64); ff[s] = *(const half8*)(rd1 + s * 64); }
            cfence();
            if (tid < 184) {
                const int it = jj * 184 + tid;
                gptr p = it < 3584 ? ((it & 4) ? w_up : w_gate) + (size_t)(it >> 3) * F_ + xg * 224 + (it & 3) * 64
                                         : w_down + (size_t)xg * 224 * D_ + (it - 3584) * 64;
                warm = *(const GAS unsigned*)p;
            }
            st16(rs, WS_H2 + pair * 8192 + tid * 16, ones);
            if (tid < 128) st16(rs, WS_PART + pair * 2048 + tid * 16, ones);
            else if (tid < 130 && pair < 129) st16(rs, WS_R + pair * 32 + (tid - 128) * 16, (u32x4){0, 0, 0, 0});
            cfence();
            {
                cf g1 = cexp_pow2(t2, rh, rl, 256.f), g2 = csqr(g1), g3 = cmul(g2, g1), g4 = csqr(g2);
                st4rows<0>(wba, pkc(g1), pkc(cmul(g1, p2)), pkc(cmul(g1, p4)), pkc(cmul(g1, p6)));
                st4rows<4 * KG_ROW>(wba, pkc(cmul(g1, p8)), pkc(cmul(g1, p10)), pkc(cmul(g1, p12)), pkc(cmul(g1, p14)));
                st4rows<8 * KG_ROW>(wba, pkc(g2), pkc(cmul(g2, p4)), pkc(cmul(g2, p8)), pkc(cmul(g2, p12)));
                st4rows<12 * KG_ROW>(wba, pkc(g3), pkc(cmul(g3, p4)), pkc(cmul(g3, p8)), pkc(cmul(g3, p12)));
                cf g5 = cmul(g4, g1), g6 = cmul(g4, g2), g7 = cmul(g4, g3), g8 = csqr(g4);
                st4rows<KG_BUF>(wba, pkc(g4), pkc(cmul(g4, p8)), pkc(g5), pkc(cmul(g5, p8)));
                st4rows<KG_BUF + 4 * KG_ROW>(wba, pkc(g6), pkc(cmul(g6, p8)), pkc(g7), pkc(cmul(g7, p8)));
                *(unsigned*)(wb + KG_BUF + 8 * KG_ROW + lane * 4) = pkc(g8);
            }
#pragma unroll
            for (int s = 0; s < 4; ++s) acc0 = MFMA16(xf[s], ff[s], acc0);
            cfence();
#pragma unroll
            for (int s = 0; s < 4; ++s) {
                acc1 = MFMA16(xf[s], *(const half8*)(rd0 + s * 64), acc1);
                acc2 = MFMA16(xf[s], *(const half8*)(rd1 + s * 64), acc2);
            }
            cfence();
            if (w == 7) mpv = ld8(rs, mpoff);
            const float us = pow2i(ew - 14);
            acc0 *= us; acc1 *= us; acc2 *= us;
            if (rho > 0.f) {
                const float rw = rho * 1.44269504088896341f;
                const int h1 = fr < 8 ? 256 + 2 * fr : 512 + 256 * ((fr - 8) >> 2) + 4 * (fr & 3);
                const int h2 = fr < 8 ? 1024 + 256 * (fr >> 1) + 8 * (fr & 1) : 2048;
#pragma unroll 1
                for (int j = 0; j < 4; ++j) {
                    const int lo16 = 16 * (4 * fq + j);
                    const float e0 = exp2f(rw * (float)(fr + lo16)), e1 = exp2f(rw * (float)(h1 + lo16));
                    const float e2 = exp2f(rw * (float)(h2 + lo16 > 2048 ? 2048 : h2 + lo16));
#pragma unroll
                    for (int q = 0; q < 4; ++q)
                        if (q == j) { acc0[q] *= e0; acc1[q] *= e1; acc2[q] *= e2; }
                }
            }
            float* P = (float*)wb;
            *(f32x4*)(P + (0 + fr) * 20 + 4 * fq) = acc0;
            *(f32x4*)(P + (16 + fr) * 20 + 4 * fq) = acc1;
            *(f32x4*)(P + (32 + fr) * 20 + 4 * fq) = acc2;
            const float eb = (float)ew + __builtin_fminf(__builtin_ceilf(rho * (2048.f * 1.44269504088896341f)), 100.f);
            if (lane == 0) misc[w] = eb;
        }
        STAMP(4);
        bar();
        int eM0, eM1;
        {
            const f32x4 m0 = *(const f32x4*)misc, m1 = *(const f32x4*)(misc + 4);
            eM0 = (int)__builtin_fminf(__builtin_fmaxf(__builtin_fmaxf(m0[0], m0[1]), __builtin_fmaxf(m0[2], m0[3])), 110.f);
            eM1 = (int)__builtin_fminf(__builtin_fmaxf(__builtin_fmaxf(m1[0], m1[1]), __builtin_fmaxf(m1[2], m1[3])), 110.f);
        }
        {
            const int th = tid & 255;
            unsigned char* T16 = smem + SM_T16 + c * 1600;
            if (th < 192) {
                const float* P = (const float*)(smem + SM_KG + c * 4 * KG_WAVE) + (th >> 2) * 20 + 4 * (th & 3);
                f32x4 s = *(const f32x4*)P + *(const f32x4*)(P + KG_WAVE / 4);
                s += *(const f32x4*)(P + 2 * (KG_WAVE / 4)) + *(const f32x4*)(P + 3 * (KG_WAVE / 4));
                s *= pow2i(6 - (c ? eM1 : eM0));
                *(u32x2*)(T16 + th * 8) = (u32x2){pk_f16(s[0], s[1]), pk_f16(s[2], s[3])};
            } else {
                if (th == 192) *(unsigned*)(T16 + 1536) = 0;
                *(u32x2*)(smem + SM_U + c * (U_LEN * 2) + lane * 8) = (u32x2){0, 0};
                if (w == 7) {
#pragma unroll 1
                    for (unsigned spins = 0; spins < SPIN_CAP; ++spins) {
                        if (wave_all(mpv[1] == tag || (mpl & 7) == 7)) break;
                        OPAQUE(mpoff);
                        mpv = ld8(rs, mpoff);
                    }
                    float s = (mpl & 7) == 7 ? 0.f : u2f(mpv[0]);
                    s += dpp_f<0xB1>(s); s += dpp_f<0x4E>(s); s += dpp_f<0x141>(s);
                    if (lane < 48 && (lane & 7) == 0) misc[16 + (lane >> 3)] = bfr(s);
                }
            }
        }
        bar();
        {
            const unsigned char* T16 = smem + SM_T16 + c * 1600;
            const unsigned e0 = *(const u16*)(T16 + (kix0[0] & 0xffff)), e1 = *(const u16*)(T16 + (kix0[0] >> 16));
            const unsigned e2 = *(const u16*)(T16 + (kix0[1] & 0xffff)), e3 = *(const u16*)(T16 + (kix0[1] >> 16));
            const unsigned e4 = *(const u16*)(T16 + (kix1[0] & 0xffff)), e5 = *(const u16*)(T16 + (kix1[0] >> 16));
            const unsigned e6 = *(const u16*)(T16 + (kix1[1] & 0xffff));
            const unsigned p01 = e0 | (e1 << 16), p12 = e1 | (e2 << 16), p23 = e2 | (e3 << 16);
            const unsigned p34 = e3 | (e4 << 16), p45 = e4 | (e5 << 16), p56 = e5 | (e6 << 16);
            unsigned char* kc = smem + SM_KC + c * 4 * KC_STR + (tid & 255) * 8;
            *(u32x2*)(kc) = (u32x2){p01, p23};
            *(u32x2*)(kc + KC_STR) = (u32x2){p12, p34};
            *(u32x2*)(kc + 2 * KC_STR) = (u32x2){p23, p45};
            *(u32x2*)(kc + 3 * KC_STR) = (u32x2){p34, p56};
        }
        bar();
        STAMP(5);
        {
            const unsigned char* kcb = smem + SM_KC + c * 4 * KC_STR + ((3 - fr) & 3) * KC_STR;
            const unsigned char* kb1 = kcb + ((31 + koffb - fr) & ~3) * 2;
            const unsigned char* kb2 = kcb + ((15 - fr + 4 * fq) & ~3) * 2;
            if (hs == 0) conv_kfrags<0>(kf, kb1, kb2, fr, koffb); else conv_kfrags<1>(kf, kb1, kb2, fr, koffb);
#pragma unroll
            for (int i = 0; i < 4; ++i) { PIN(kf.a[i]); PIN(kf.b[i]); PIN(kf.x[i]); }
#pragma unroll
            for (int i = 0; i < 3; ++i) PIN(kf.c[i]);
        }
        const f32x4 mA = *(const f32x4*)(misc + 16);
        const float ga0 = misc[20], ga1 = misc[21];
        const float opc0 = bfr(1.f + mA[2]), opc1 = bfr(1.f + mA[3]);
        const int eu0 = exp_above(32.f * __builtin_fabsf(opc0) + __builtin_fabsf(mA[0]));
        const int eu1 = exp_above(32.f * __builtin_fabsf(opc1) + __builtin_fabsf(mA[1]));
        const float su0 = pow2i(11 - eu0), su1 = pow2i(11 - eu1), iu0 = pow2i(eu0 - 11), iu1 = pow2i(eu1 - 11);
        float oc0 = opc0 * su0, oc1 = opc1 * su1, sa0 = mA[0] * su0, sa1 = mA[1] * su1;
        float dk0 = bflo(dsk2) * iu0, dk1 = bfhi(dsk2) * iu1;
        float ys0 = pow2i(eM0 - 6) * iu0, ys1 = pow2i(eM1 - 6) * iu1;
        PIN(oc0); PIN(oc1); PIN(sa0); PIN(sa1); PIN(dk0); PIN(dk1); PIN(ys0); PIN(ys1); PIN(warm);
        STAMP(6);
        u32x4 g0, g1;
        {
            unsigned off = WS_XG + pair * 16384 + tid * 32;
#pragma unroll 1
            for (unsigned spins = 0; spins < SPIN_CAP; ++spins) {
                OPAQUE(off);
                g0 = ld16(rs, off);
                g1 = ld16(rs, off + 16);
                if (wave_all(((g0[1] ^ tag) | (g0[3] ^ tag) | (g1[1] ^ tag) | (g1[3] ^ tag)) == 0)) break;
            }
        }
        STAMP(7);
        m2v = ld16(rs, m2off);
        unsigned hd[4];
        {
            const unsigned xn[4] = {g0[0], g0[2], g1[0], g1[2]};
            float h0[4], h1[4];
#pragma unroll
            for (int j = 0; j < 4; ++j) {
                const unsigned a = pk_bf16(bflo(xn[j]) * oc0, bfhi(xn[j]) * oc1);
                const unsigned h = pk_bf16(bflo(a) + sa0, bfhi(a) + sa1);
                h0[j] = bflo(h); h1[j] = bfhi(h);
            }
            unsigned char* U0 = smem + SM_U + (U_PAD + 4 * tid) * 2;
            *(u32x2*)(U0) = (u32x2){pk_f16(h0[0], h0[1]), pk_f16(h0[2], h0[3])};
            *(u32x2*)(U0 + U_LEN * 2) = (u32x2){pk_f16(h1[0], h1[1]), pk_f16(h1[2], h1[3])};
            bar();
#pragma unroll
            for (int j = 0; j < 4; ++j) hd[j] = pk_bf16(h0[j] * dk0, h1[j] * dk1);
        }
        STAMP(8);
        if (b == 0 && tid == 0) st4(rs, WS_E, tag);
        {
            const unsigned char* Uc = smem + SM_U + c * (U_LEN * 2) + (U_PAD + 16 * fr + koffb) * 2;
            f32x4* red = (f32x4*)(smem + SM_KG) + (c * 2 + hs) * 512 + 4 * fr + (fq ^ ((fr >> 1) & 3));
            if (hs == 0) { if (tg == 0) conv_mfma<0, 0>(Uc, kf, red); else conv_mfma<0, 1>(Uc, kf, red); }
            else         { if (tg == 0) conv_mfma<1, 0>(Uc, kf, red); else conv_mfma<1, 1>(Uc, kf, red); }
        }
        STAMP(9);
        bar();
        {
            const f32x4* red = (const f32x4*)(smem + SM_KG) + ((tid & ~3) | ((tid ^ (tid >> 3)) & 3));
            const f32x4 y0 = (red[0] + red[512]) * ys0;
            const f32x4 y1 = (red[1024] + red[1536]) * ys1;
            u32x4 o;
#pragma unroll
            for (int j = 0; j < 4; ++j) {
                const unsigned y = pk_bf16(y0[j], y1[j]);
                const unsigned z = pk_bf16(bflo(y) + bflo(hd[j]), bfhi(y) + bfhi(hd[j]));
                o[j] = umin_(pk_bf16(ga0 * bflo(z), ga1 * bfhi(z)), 0xfffffffeu);
            }
            st16(rs, WS_W + ((tid >> 3) * 224 + pair) * 128 + (tid & 7) * 16, o);
        }
        STAMP(10);
    } else {
        mod_unit_a(um2, (u16)p_are, mred + 512, lane, w);
        mod_unit_a(um3, (u16)p_aim, mred + 1024, lane, w);
        bar();
        if (w == 6) mod_unit_b(mred + 512, cg2, rg2, tag, rs, lane);
        if (w == 7) mod_unit_b(mred + 1024, cg3, rg3, tag, rs, lane);
        if (w < 6) {
            const int l = tid < 336 ? tid : 0;
            unsigned off = WS_MP + (3 * D_ + fb * 42) * 64 + l * 8;
            u32x2 v;
#pragma unroll 1
            for (unsigned spins = 0; spins < SPIN_CAP; ++spins) {
                OPAQUE(off);
                v = ld8(rs, off);
                if (wave_all(v[1] == tag || (l & 7) == 7)) break;
                __builtin_amdgcn_s_sleep(2);
            }
            float s = (l & 7) == 7 ? 0.f : u2f(v[0]);
            s += dpp_f<0xB1>(s); s += dpp_f<0x4E>(s); s += dpp_f<0x141>(s);
            const float s2 = dpp_f<0x128>(s);
            if (tid < 336 && (tid & 15) == 0) st8(rs, WS_MOD2 + (fb * 21 + (tid >> 4)) * 8, (u32x2){pk_bf16(s, s2), tag});
        }
        STAMP(4);
#pragma unroll 1
        for (int base = w * 64; base < 6272; base += 512) {
            const int ch = base + lane, mat = ch >= 3136, cc = ch - mat * 3136, row = cc / 7, kk = cc - row * 7;
            __builtin_amdgcn_global_load_lds((const GAS unsigned*)((mat ? w_up : w_gate) + (size_t)row * F_ + fb * 56 + kk * 8),
                                             (LAS unsigned*)(smem + SM_KG + base * 16), 16, 0, 0);
        }
        if (tid < 128) *(u32x4*)(smem + SM_SEL + tid * 16) = (u32x4){0, 0, 0, 0};
        asm volatile("s_waitcnt vmcnt(0)" ::: "memory");
        bar();
        STAMP(5);
        const int wq = w < 7 ? w : 0;
        {
            const unsigned char* p = smem + SM_KG + ((fr >> 1) & 1) * 50176 + (8 * fq + (fr >> 2)) * 112 + (8 * wq + 4 * (fr & 1)) * 2;
#pragma unroll
            for (int ks = 0; ks < 14; ++ks) {
                const u32x2 a = tr_read(p + ks * 32 * 112), c2 = tr_read(p + ks * 32 * 112 + 4 * 112);
                fgu[ks] = (u32x4){a[0], a[1], c2[0], c2[1]};
            }
        }
        bar();
#pragma unroll 1
        for (int base = w * 64; base < 3136; base += 512)
            __builtin_amdgcn_global_load_lds((const GAS unsigned*)(w_down + (size_t)fb * 56 * D_ + (base + lane) * 8),
                                             (LAS unsigned*)(smem + SM_KG + base * 16), 16, 0, 0);
        asm volatile("s_waitcnt vmcnt(0)" ::: "memory");
        bar();
#pragma unroll
        for (int ks = 0; ks < 2; ++ks)
#pragma unroll
            for (int i = 0; i < 4; ++i) {
                int r0 = 32 * ks + 8 * fq + (fr >> 2), r1 = r0 + 4;
                r0 = r0 > 55 ? 55 : r0; r1 = r1 > 55 ? 55 : r1;
                const unsigned char* p = smem + SM_KG + (16 * (4 * wq + i) + 4 * (fr & 3)) * 2;
                const u32x2 a = tr_read(p + r0 * 896), c2 = tr_read(p + r1 * 896);
                fdn[ks * 4 + i] = (u32x4){a[0], a[1], c2[0], c2[1]};
            }
        m2v = ld16(rs, m2off);
        STAMP(10);
    }

    if (w < 6) {
        unsigned off = m2off;
#pragma unroll 1
        for (unsigned spins = 0; spins < SPIN_CAP; ++spins) {
            if (wave_all(((m2v[1] ^ tag) | (m2v[3] ^ tag)) == 0)) break;
            __builtin_amdgcn_s_sleep(2);
            OPAQUE(off);
            m2v = ld16(rs, off);
        }
        f32x4 o = {bflo(m2v[0]), bfhi(m2v[0]), bflo(m2v[2]), bfhi(m2v[2])};
        if (tid >= 112 && tid < 224) {
            const unsigned a = pk_bf16(1.f + o[0], 1.f + o[1]), c2 = pk_bf16(1.f + o[2], 1.f + o[3]);
            o = (f32x4){bflo(a), bfhi(a), bflo(c2), bfhi(c2)};
        }
        if (tid < 336) *(f32x4*)(smem + SM_PRM + tid * 16) = o;
    }
    STAMP(11);
    if (w < 7) {
        u32x4 v;
        unsigned off = WS_W + ((g >> 2) * 224 + (tid >> 1)) * 128 + (g & 3) * 32 + (tid & 1) * 16;
#pragma unroll 1
        for (unsigned spins = 0; spins < SPIN_CAP; ++spins) {
            OPAQUE(off);
            v = ld16(rs, off);
            if (wave_all(valid4(v))) break;
            if (!is_conv) __builtin_amdgcn_s_sleep(4);
        }
        unsigned* tile = (unsigned*)(smem + SM_TILE) + (tid & 1) * 4 * 228 + (tid >> 1);
#pragma unroll
        for (int j = 0; j < 4; ++j) tile[j * 228] = v[j];
    }
    STAMP(12);
    bar();
    u32x4 orow;
    {
        const u32x4 wv = *(const u32x4*)(smem + SM_TILE + (w * 228 + lo * 4) * 4);
        float sbf[8], ocf[8], gbf[8];
        {
            const f32x4* pp = (const f32x4*)(smem + SM_PRM) + lo * 2;
#pragma unroll
            for (int h = 0; h < 2; ++h) {
                const f32x4 a = pp[h], c2 = pp[112 + h], d2 = pp[224 + h];
#pragma unroll
                for (int j = 0; j < 4; ++j) { sbf[4 * h + j] = a[j]; ocf[4 * h + j] = c2[j]; gbf[4 * h + j] = d2[j]; }
            }
        }
        float x1[8];
#pragma unroll
        for (int j = 0; j < 4; ++j) {
            const unsigned a = pk_bf16(xf32[2 * j] + bflo(wv[j]), xf32[2 * j + 1] + bfhi(wv[j]));
            x1[2 * j] = bflo(a); x1[2 * j + 1] = bfhi(a);
        }
        float mb, rinv;
        ln_stats(x1, act, mb, rinv);
        u32x4 hp;
        float r = 0.f;
#pragma unroll
        for (int j = 0; j < 4; ++j) {
            const unsigned dd = pk_bf16(x1[2 * j] - mb, x1[2 * j + 1] - mb);
            const unsigned xn = pk_bf16(bflo(dd) * rinv, bfhi(dd) * rinv);
            const unsigned a = pk_bf16(bflo(xn) * ocf[2 * j], bfhi(xn) * ocf[2 * j + 1]);
            hp[j] = pk_bf16(bflo(a) + sbf[2 * j], bfhi(a) + sbf[2 * j + 1]);
            r = dot2bf(hp[j], wrv[j], r);
        }
        r = wave_sum(act ? r : 0.f);
        const unsigned rb = pk_bf16(r, r) & 0xffffu;
        const unsigned mono = (rb & 0x8000u) ? (~rb & 0xffffu) : (rb | 0x8000u);
        if (lane == 0) *(u16*)(smem + SM_MISC + 256 + w * 2) = (u16)umax_(mono, 1u);
        bar();
        if (tid == 0) st16(rs, WS_R + ((g & 7) * 32 + (g >> 3)) * 16, *(const u32x4*)(smem + SM_MISC + 256));
        STAMP(13);
        cfence();
        {
            u32x4 hq;
#pragma unroll
            for (int j = 0; j < 4; ++j) hq[j] = umin_(hp[j], 0xfffffffeu);
            if (act) st16(rs, WS_H2 + t * 896 + lane * 16, hq);
        }
        cfence();
#pragma unroll
        for (int j = 0; j < 4; ++j) {
            const unsigned a = pk_bf16(gbf[2 * j] * bflo(hp[j]), gbf[2 * j + 1] * bfhi(hp[j]));
            orow[j] = pk_bf16(x1[2 * j] + bflo(a), x1[2 * j + 1] + bfhi(a));
        }
        if (is_conv) {
            unsigned* lsel = (unsigned*)(smem + SM_SEL);
            if (w == 0) {
                unsigned off = WS_SEL + (lane & 7) * 4, sv;
#pragma unroll 1
                for (unsigned spins = 0; spins < SPIN_CAP; ++spins) {
                    __builtin_amdgcn_s_sleep(6);
                    OPAQUE(off);
                    sv = ld4(rs, off);
                    if (wave_all(sv != 0u)) break;
                }
                if (lane < 8) lsel[lane] = sv;
            }
            bar();
            const unsigned sk = lsel[lane & 7];
            if (wave_all((sk & 0xffffu) != (unsigned)(2047 - t)) && act) st16(ro, t * 896 + lane * 16, orow);
            STAMP(14);
            STAMP_FLUSH;
            return;
        }
    }

    unsigned* slots = (unsigned*)(smem + SM_SEL);
    unsigned* wmax = slots + 64;
    unsigned* sel = slots + 72;
    unsigned char* atile = smem + SM_SEL + 512;
    unsigned k4[4];
    {
        u32x2 rv;
        unsigned off = WS_R + w * 512 + lane * 8;
#pragma unroll 1
        for (unsigned spins = 0; spins < SPIN_CAP; ++spins) {
            OPAQUE(off);
            rv = ld8(rs, off);
            if (wave_all(rv[0] != 0u && rv[1] != 0u)) break;
        }
        const unsigned tb = 2047u - (unsigned)(64 * (lane >> 1) + 8 * w + 4 * (lane & 1));
        k4[0] = (rv[0] << 16) | tb; k4[1] = (rv[0] & 0xffff0000u) | (tb - 1u);
        k4[2] = (rv[1] << 16) | (tb - 2u); k4[3] = (rv[1] & 0xffff0000u) | (tb - 3u);
    }
    STAMP(15);
    {
        const unsigned m = wave_umax(umax_(umax_(k4[0], k4[1]), umax_(k4[2], k4[3])));
        if (lane == 0) wmax[w] = m;
    }
    bar();
    {
        const u32x4 a = *(const u32x4*)wmax, c2 = *(const u32x4*)(wmax + 4);
        const unsigned tau = umin_(umin_(umin_(a[0], a[1]), umin_(a[2], a[3])), umin_(umin_(c2[0], c2[1]), umin_(c2[2], c2[3])));
        unsigned long long mk[4];
        int cnt = 0;
#pragma unroll
        for (int j = 0; j < 4; ++j) { mk[j] = __builtin_amdgcn_ballot_w64(k4[j] >= tau); cnt += __builtin_popcountll(mk[j]); }
        if (cnt <= 8) {
            int base = 0;
#pragma unroll
            for (int j = 0; j < 4; ++j) {
                const int pos = base + (int)__builtin_amdgcn_mbcnt_hi((unsigned)(mk[j] >> 32), __builtin_amdgcn_mbcnt_lo((unsigned)mk[j], 0u));
                if (k4[j] >= tau) slots[w * 8 + pos] = k4[j];
                base += __builtin_popcountll(mk[j]);
            }
        } else {
#pragma unroll 1
            for (int it = 0; it < 8; ++it) {
                const unsigned m = wave_umax(umax_(umax_(k4[0], k4[1]), umax_(k4[2], k4[3])));
#pragma unroll
                for (int j = 0; j < 4; ++j) k4[j] = k4[j] == m ? 0u : k4[j];
                if (lane == 0) slots[w * 8 + it] = m;
            }
        }
    }
    bar();
    {
        const unsigned key = slots[tid >> 3];
        const u32x4 a = *(const u32x4*)(slots + 8 * (tid & 7)), c2 = *(const u32x4*)(slots + 8 * (tid & 7) + 4);
        unsigned rank = (a[0] > key) + (a[1] > key) + (a[2] > key) + (a[3] > key) + (c2[0] > key) + (c2[1] > key) + (c2[2] > key) + (c2[3] > key);
        rank += dpp_u<0xB1>(rank); rank += dpp_u<0x4E>(rank); rank += dpp_u<0x141>(rank);
        if ((tid & 7) == 0 && key != 0u && rank < 8u) sel[rank] = key;
    }
    bar();
    STAMP(16);
    if (fb == 0 && tid < 8) st4(rs, WS_SEL + tid * 4, sel[tid]);
    const int tokF = 4 * (lane & 1) + (lane & 2) + ((lane >> 4) & 1);
    const unsigned keyF = sel[tokF];
    const int tW = 2047 - (int)(sel[w] & 0xffffu), tF = 2047 - (int)(keyF & 0xffffu);
    const int pc = 7 * fb + (w < 7 ? w : 0);
    unsigned char* h2t = smem + SM_TILE;
    {
        u32x4 v;
        unsigned off = WS_H2 + tW * 896 + lo * 16;
#pragma unroll 1
        for (unsigned spins = 0; spins < SPIN_CAP; ++spins) {
            OPAQUE(off);
            v = ld16(rs, off);
            if (wave_all(valid4(v))) break;
        }
        if (act) *(u32x4*)(h2t + w * 912 + lane * 16) = v;
    }
    if (wave_all(tF != t) && act) st16(ro, t * 896 + lane * 16, orow);
    const unsigned fx = *(const GAS unsigned*)(x + (size_t)tF * D_ + 2 * pc);
    const unsigned fw = ld4(rs, WS_W + ((tF >> 5) * 224 + pc) * 128 + (tF & 31) * 4);
    STAMP(17);
    bar();
    if (w < 7) {
        f32x4 acc = {0, 0, 0, 0}, acb = {0, 0, 0, 0};
        const unsigned char* ap = h2t + (lane & 7) * 912 + fq * 16;
#pragma unroll
        for (int ks = 0; ks < 14; ks += 2) {
            acc = MFMAB(*(const bf16x8*)(ap + ks * 64), __builtin_bit_cast(bf16x8, fgu[ks]), acc);
            acb = MFMAB(*(const bf16x8*)(ap + ks * 64 + 64), __builtin_bit_cast(bf16x8, fgu[ks + 1]), acb);
        }
        acc += acb;
        const int idx = 2 * (fr >> 3) + (fq >> 1);
        const float lo2 = (idx & 1) ? acc[1] : acc[0], hi2 = (idx & 1) ? acc[3] : acc[2];
        const float keep = (idx & 2) ? hi2 : lo2, send = (idx & 2) ? lo2 : hi2;
        const float recv = dpp_f<0x128>(send);
        const float gg = bfr(fr < 8 ? keep : recv), uu = bfr(fr < 8 ? recv : keep);
        const float sg = bfr(gg * frcp(1.f + fexp(-gg)));
        *(u16*)(atile + (4 * (fq & 1) + idx) * 144 + (8 * w + (fr & 7)) * 2) = (u16)pk_bf16(sg * uu, 0.f);
    }
    bar();
    STAMP(18);
    if (w < 7) {
        const bf16x8 a0 = *(const bf16x8*)(atile + (lane & 7) * 144 + fq * 16);
        const bf16x8 a1 = *(const bf16x8*)(atile + (lane & 7) * 144 + 64 + fq * 16);
        f32x4 acc[4];
#pragma unroll
        for (int i = 0; i < 4; ++i) acc[i] = MFMAB(a0, __builtin_bit_cast(bf16x8, fdn[i]), ((f32x4){0, 0, 0, 0}));
#pragma unroll
        for (int i = 0; i < 4; ++i) acc[i] = MFMAB(a1, __builtin_bit_cast(bf16x8, fdn[4 + i]), acc[i]);
        {
#pragma unroll
            for (int i = 0; i < 2; ++i) {
                u32x4 o;
#pragma unroll
                for (int j = 0; j < 4; ++j) o[j] = umin_(f2u(fq < 2 ? acc[i][j] : acc[2 + i][j]), 0xfffffffeu);
                st16(rs, WS_PART + ((16 * (4 * w + i + (fq & 2)) + fr) * 32 + fb) * 32 + (fq & 1) * 16, o);
            }
        }
        STAMP(19);
        const int hi = lane >> 5;
        const unsigned mono = keyF >> 16;
        const unsigned rb = (mono & 0x8000u) ? (mono & 0x7fffu) : (~mono & 0xffffu);
        float rsel = bfr(frcp(1.f + fexp(-u2f(rb << 16))));
        float x1 = bfr((hi ? bfhi(fx) : bflo(fx)) + (hi ? bfhi(fw) : bflo(fw)));
        float h2v = bf2f(*(const u16*)(h2t + tokF * 912 + (2 * pc + hi) * 2));
        float gbv = ((const float*)(smem + SM_PRM))[896 + 2 * pc + hi];
        PIN(rsel); PIN(x1); PIN(h2v); PIN(gbv);
        u32x4 c0, c1;
        unsigned off = WS_PART + (2 * pc) * 1024 + lane * 16;
#pragma unroll 1
        for (unsigned spins = 0; spins < SPIN_CAP; ++spins) {
            OPAQUE(off);
            c0 = ld16(rs, off);
            c1 = ld16(rs, off + 1024);
            if (wave_all(valid4(c0) && valid4(c1))) break;
        }
        STAMP(20);
        float v[4];
#pragma unroll
        for (int j = 0; j < 4; ++j) v[j] = fold32(u2f(c0[j]), u2f(c1[j]));
        float u0 = fold16(v[0], v[1]), u1 = fold16(v[2], v[3]);
        u0 += dpp_f<0x128>(u0); u1 += dpp_f<0x128>(u1);
        u0 += dpp_f<0x124>(u0); u1 += dpp_f<0x124>(u1);
        u0 += dpp_f<0x4E>(u0);  u1 += dpp_f<0x4E>(u1);
        const float s = (lane & 2) ? u1 : u0;
        const float y2 = bfr(bfr(s) * rsel);
        const float bb = bfr(h2v + y2);
        const float o = x1 + bfr(gbv * bb);
        if ((lane & 12) == 0) st2(ro, tF * 896 + (2 * pc + hi) * 2, pk_bf16(o, o) & 0xffffu);
        STAMP(21);
    }
    asm volatile(".Lcode_mark_b:");
    STAMP_FLUSH;
}

extern "C" void kernel_launch(void* const* d_in, const int* in_sizes, int n_in,
                              void* d_out, int out_size, void* d_ws, size_t ws_size,
                              hipStream_t stream) {
    fused_block<<<256, 512, 0, stream>>>(
        (const u16*)d_in[0], (const u16*)d_in[1], (const u16*)d_in[2], (const u16*)d_in[3], (const u16*)d_in[4],
        (const u16*)d_in[5], (const u16*)d_in[6], (const u16*)d_in[7], (const u16*)d_in[8], (const u16*)d_in[10],
        (const u16*)d_in[11], (const u16*)d_in[12], (const u16*)d_in[13], (const u16*)d_in[14], (const u16*)d_in[15],
        (unsigned char*)d_ws, (u16*)d_out);
}
```

```cpp
#include <hip/hip_runtime.h>
#include <stdint.h>

typedef _Float16 half8 __attribute__((ext_vector_type(8)));
typedef _Float16 half2v __attribute__((ext_vector_type(2)));
typedef __bf16 bf16x8 __attribute__((ext_vector_type(8)));
typedef __bf16 bf16x2 __attribute__((ext_vector_type(2)));
typedef float f32x4 __attribute__((ext_vector_type(4)));
typedef unsigned u32x4 __attribute__((ext_vector_type(4)));
typedef unsigned u32x2 __attribute__((ext_vector_type(2)));
typedef unsigned short u16;
#define NOPK
#define DEV NOPK __device__ __forceinline__

constexpr int L_ = 2048, D_ = 448, N_ = 256, F_ = 1792;

constexpr unsigned WS_E    = 0;
constexpr unsigned WS_XG   = 4096;
constexpr unsigned WS_W    = WS_XG + 224u * 2048u * 8u;
constexpr unsigned WS_H2   = WS_W + L_ * D_ * 2;
constexpr unsigned WS_PART = WS_H2 + L_ * D_ * 2;
constexpr unsigned WS_R    = WS_PART + 448u * 32u * 32u;
constexpr unsigned WS_SEL  = WS_R + 4096;
constexpr unsigned WS_WP   = WS_SEL + 4096;
constexpr unsigned WS_MP   = WS_WP + 64u * 224u * 16u;
constexpr unsigned WS_END  = WS_MP + 2688u * 64u;

#define STAMP_DECL
#define STAMP(i)
#define STAMP_FLUSH

DEV float u2f(unsigned u) { return __builtin_bit_cast(float, u); }
DEV unsigned f2u(float f) { return __builtin_bit_cast(unsigned, f); }
DEV void bar() { asm volatile("s_waitcnt lgkmcnt(0)\n\ts_barrier" ::: "memory"); }
DEV void cfence() { asm volatile("" ::: "memory"); }
DEV bool wave_all(bool p) { return __builtin_amdgcn_ballot_w64(!p) == 0; }
DEV float bf2f(u16 v) { return u2f((unsigned)v << 16); }
DEV float bflo(unsigned v) { return u2f(v << 16); }
DEV float bfhi(unsigned v) { return u2f(v & 0xffff0000u); }
typedef float f32x2 __attribute__((ext_vector_type(2)));
DEV unsigned pk_bf16(float a, float b) {
    const f32x2 v = {a, b};
    unsigned r = __builtin_bit_cast(unsigned, __builtin_convertvector(v, bf16x2));
    asm("" : "+v"(r));
    return r;
}
DEV float bfr(float x) { return bfhi(pk_bf16(x, x)); }
DEV unsigned pk_f16(float a, float b) {
    asm("" : "+v"(a), "+v"(b));
    const f32x2 v = {a, b};
    return __builtin_bit_cast(unsigned, __builtin_convertvector(v, half2v));
}
DEV unsigned pk_f16n(float a, float b) {
    asm("" : "+v"(a), "+v"(b));
    const f32x2 v = {a, -b};
    return __builtin_bit_cast(unsigned, __builtin_convertvector(v, half2v));
}
DEV unsigned pk_f16m(float a, float b) {
    const f32x2 v = {a, b};
    return __builtin_bit_cast(unsigned, __builtin_convertvector(v, half2v));
}
DEV float dot2bf(unsigned a, unsigned b, float c) {
    return __builtin_amdgcn_fdot2_f32_bf16(__builtin_bit_cast(bf16x2, a), __builtin_bit_cast(bf16x2, b), c, false);
}
DEV unsigned umax_(unsigned a, unsigned b) { return a > b ? a : b; }
DEV unsigned umin_(unsigned a, unsigned b) { return a < b ? a : b; }
template <int CTRL> DEV unsigned dpp_u(unsigned v) {
    return (unsigned)__builtin_amdgcn_update_dpp(0, (int)v, CTRL, 0xf, 0xf, true);
}
template <int CTRL> DEV float dpp_f(float v) { return u2f(dpp_u<CTRL>(f2u(v))); }
DEV float row_sum(float v) {
    v += dpp_f<0xB1>(v);
    v += dpp_f<0x4E>(v);
    v += dpp_f<0x141>(v);
    v += dpp_f<0x140>(v);
    return v;
}
DEV float fold32(float a, float b) {
    auto r = __builtin_amdgcn_permlane32_swap(f2u(a), f2u(b), false, false);
    return u2f(r[0]) + u2f(r[1]);
}
DEV float fold16(float a, float b) {
    auto r = __builtin_amdgcn_permlane16_swap(f2u(a), f2u(b), false, false);
    return u2f(r[0]) + u2f(r[1]);
}
DEV float rdlane(float v, int l) { return u2f((unsigned)__builtin_amdgcn_readlane((int)f2u(v), l)); }
DEV void wave_sum2(float a, float b, float& sa, float& sb) {
    float v = fold32(a, b);
    v = fold16(v, v);
    v = row_sum(v);
    sa = rdlane(v, 0); sb = rdlane(v, 32);
}
DEV float wave_sum(float a) {
    float v = fold32(a, a);
    v = fold16(v, v);
    return row_sum(v);
}
DEV unsigned wave_umax(unsigned v) {
    v = umax_(v, dpp_u<0xB1>(v));
    v = umax_(v, dpp_u<0x4E>(v));
    v = umax_(v, dpp_u<0x141>(v));
    v = umax_(v, dpp_u<0x140>(v));
    { auto r = __builtin_amdgcn_permlane16_swap(v, v, false, false); v = umax_(r[0], r[1]); }
    { auto r = __builtin_amdgcn_permlane32_swap(v, v, false, false); v = umax_(r[0], r[1]); }
    return v;
}
DEV int exp_above(float m) {
    int e = (int)((f2u(m) >> 23) & 0xff) - 126;
    return e < -100 ? -100 : (e > 100 ? 100 : e);
}
DEV float pow2i(int e) { return u2f((unsigned)(e + 127) << 23); }
DEV float fexp(float x) { return __builtin_amdgcn_exp2f(x * 1.44269504088896341f); }
DEV float frcp(float x) { return __builtin_amdgcn_rcpf(x); }
DEV bool valid4(u32x4 v) { return umax_(umax_(v[0], v[1]), umax_(v[2], v[3])) != 0xffffffffu; }

typedef __amdgpu_buffer_rsrc_t rsrc_t;
DEV u32x4 ld16(rsrc_t r, unsigned off) { return __builtin_amdgcn_raw_buffer_load_b128(r, off, 0, 16); }
DEV u32x2 ld8(rsrc_t r, unsigned off) { return __builtin_amdgcn_raw_buffer_load_b64(r, off, 0, 16); }
DEV unsigned ld4(rsrc_t r, unsigned off) { return __builtin_amdgcn_raw_buffer_load_b32(r, off, 0, 16); }
DEV void st16(rsrc_t r, unsigned off, u32x4 v) { __builtin_amdgcn_raw_buffer_store_b128(v, r, off, 0, 16); }
DEV void st8(rsrc_t r, unsigned off, u32x2 v) { __builtin_amdgcn_raw_buffer_store_b64(v, r, off, 0, 16); }
DEV void st4(rsrc_t r, unsigned off, unsigned v) { __builtin_amdgcn_raw_buffer_store_b32(v, r, off, 0, 16); }
DEV void st2(rsrc_t r, unsigned off, unsigned v) { __builtin_amdgcn_raw_buffer_store_b16((u16)v, r, off, 0, 16); }
constexpr unsigned SPIN_CAP = 1u << 15;
#define OPAQUE(v) asm volatile("" : "+v"(v) :: "memory")
#define PIN(v) asm volatile("" : "+v"(v))

constexpr int IMG_R1 = 0, IMG_RX = 560, IMG_RC = 608, IMG_RD = 864;
constexpr int lq_of(int l) {
    if (l < 256) return l;
    int s = l < 512 ? 1 : (l < 1024 ? 2 : 3);
    int q = l >> s, rem = l & ((1 << s) - 1), half = 1 << (s - 1);
    if (rem > half || (rem == half && (q & 1))) q++;
    return q << s;
}
constexpr int slot_of(int c) {
    if (c < 256) return (c & 15) * 16 + (c >> 4);
    if (c < 512) return (16 + ((c & 15) >> 1)) * 16 + ((c - 256) >> 4);
    if (c < 1024) return (24 + 4 * ((c - 512) >> 8) + ((c & 15) >> 2)) * 16 + ((c & 255) >> 4);
    if (c < 2048) return (32 + 2 * ((c - 1024) >> 8) + ((c & 15) >> 3)) * 16 + ((c & 255) >> 4);
    return 40 * 16;
}
struct KTab { u16 v[1032]; };
constexpr KTab make_ktab() {
    KTab t{};
    for (int n = 0; n < 1032; ++n) t.v[n] = 2 * 768;
    for (int n = 0; n < 560; ++n) { int l = 543 - n; if (l >= 0 && l <= 514) t.v[IMG_R1 + n] = (u16)(2 * slot_of(lq_of(l))); }
    for (int n = 0; n < 48; ++n) { int l = 1055 - n; if (l == 1027 || l == 1028) t.v[IMG_RX + n] = (u16)(2 * slot_of(1024)); }
    const int coffC[4] = {16, 4, 8, 12};
    for (int e = 0; e < 4; ++e)
        for (int n = 0; n < 64; ++n) { int lam = 79 - n; if (lam >= 32 && lam <= 63) t.v[IMG_RC + e * 64 + n] = (u16)(2 * slot_of(16 * lam + coffC[e])); }
    const int coffD[2] = {16, 8};
    for (int e = 0; e < 2; ++e)
        for (int n = 0; n < 80; ++n) { int lam = 127 - n; if (lam >= 64 && lam <= 127) t.v[IMG_RD + e * 80 + n] = (u16)(2 * slot_of(16 * lam + coffD[e])); }
    return t;
}
__device__ __attribute__((aligned(16))) const KTab g_ktab = make_ktab();

typedef f32x2 cf;
#define PKM(d, a, b) "v_pk_mul_f32 " d ", " a ", " b " op_sel_hi:[1,0]\n\t"
#define PKF(d, a, b) "v_pk_fma_f32 " d ", " a ", " b ", " d " op_sel:[1,1,0] op_sel_hi:[0,1,1] neg_lo:[1,0,0]\n\t"
DEV void cmul4(cf& r0, cf a0, cf b0, cf& r1, cf a1, cf b1, cf& r2, cf a2, cf b2, cf& r3, cf a3, cf b3) {
    asm(PKM("%0", "%4", "%5") PKM("%1", "%6", "%7") PKM("%2", "%8", "%9") PKM("%3", "%10", "%11")
        PKF("%0", "%4", "%5") PKF("%1", "%6", "%7") PKF("%2", "%8", "%9") PKF("%3", "%10", "%11")
        : "=&v"(r0), "=&v"(r1), "=&v"(r2), "=&v"(r3)
        : "v"(a0), "v"(b0), "v"(a1), "v"(b1), "v"(a2), "v"(b2), "v"(a3), "v"(b3));
}
DEV void cmul3(cf& r0, cf a0, cf b0, cf& r1, cf a1, cf b1, cf& r2, cf a2, cf b2) {
    asm(PKM("%0", "%3", "%4") PKM("%1", "%5", "%6") PKM("%2", "%7", "%8")
        PKF("%0", "%3", "%4") PKF("%1", "%5", "%6") PKF("%2", "%7", "%8")
        : "=&v"(r0), "=&v"(r1), "=&v"(r2) : "v"(a0), "v"(b0), "v"(a1), "v"(b1), "v"(a2), "v"(b2));
}
DEV cf cexp_pow2(float t2, float rh, float rl, float Lf) {
    float mag = __builtin_amdgcn_exp2f(t2 * Lf);
    float a = rh * Lf;
    float ph = (a - __builtin_rintf(a)) + rl * Lf;
    float cr = mag * __builtin_amdgcn_cosf(ph), ci = mag * __builtin_amdgcn_sinf(ph);
    asm("" : "+v"(cr), "+v"(ci));
    return (cf){cr, ci};
}
#define MFMA16(a, b, c) __builtin_amdgcn_mfma_f32_16x16x32_f16(a, b, c, 0, 0, 0)
#define MFMAB(a, b, c) __builtin_amdgcn_mfma_f32_16x16x32_bf16(a, b, c, 0, 0, 0)

DEV void ln_stats(const float (&f)[8], bool act, float& mb, float& rinv) {
    const float pv = u2f((unsigned)__builtin_amdgcn_readfirstlane((int)f2u(f[0])));
    float s1 = 0.f, s2 = 0.f;
#pragma unroll
    for (int j = 0; j < 8; ++j) { const float dd = f[j] - pv; s1 += dd; s2 = __builtin_fmaf(dd, dd, s2); }
    s1 = act ? s1 : 0.f; s2 = act ? s2 : 0.f;
    float t1, t2;
    wave_sum2(s1, s2, t1, t2);
    const float dm = t1 * (1.f / D_);
    const float var = __builtin_fmaxf(__builtin_fmaf(-dm, dm, t2 * (1.f / D_)), 0.f);
    mb = bfr(pv + dm);
    rinv = frcp(bfr(__builtin_amdgcn_sqrtf(bfr(bfr(var) + 1e-5f))));
}

constexpr int KG_ROW = 288, KG_BUF = 16 * KG_ROW, KG_WAVE = 2 * KG_BUF;
constexpr int KC_STR = 2112;
constexpr int U_PAD = 256, U_LEN = U_PAD + L_;
constexpr int SM_KG = 0;
constexpr int SM_T16 = 73728;
constexpr int SM_KC = 76928;
constexpr int SM_U = 93824;
constexpr int SM_MR = 103040;
constexpr int SM_TILE = 150528;
constexpr int SM_PRM = SM_TILE + 7296;
constexpr int SM_MISC = SM_PRM + 5376;
constexpr int SM_SEL = SM_MISC + 160;
constexpr int SM_TOTAL = SM_SEL + 320;
#define LAS __attribute__((address_space(3)))
typedef short s16x4 __attribute__((ext_vector_type(4)));
DEV u32x2 tr_read(const unsigned char* p) {
    return __builtin_bit_cast(u32x2, __builtin_amdgcn_ds_read_tr16_b64_v4i16((LAS s16x4*)p));
}
DEV half8 kfrag(const unsigned char* pa, const unsigned char* pb) {
    const u32x2 a = *(const volatile LAS u32x2*)pa, b = *(const volatile LAS u32x2*)pb;
    const u32x4 v = {a[0], a[1], b[0], b[1]};
    return __builtin_bit_cast(half8, v);
}
DEV half8 box_frag(int p, int koff, int phif, int wd) {
    int sh = p - phif - koff - (wd - 1);
    sh = sh > 31 ? 31 : sh;
    const unsigned m = (unsigned)(((unsigned long long)((1u << wd) - 1u) << (sh + 32)) >> 32);
    u32x4 v;
#pragma unroll
    for (int k = 0; k < 4; ++k) {
        const unsigned x = (m >> (2 * k)) & 3u;
        v[k] = ((x | (x << 15)) & 0x10001u) * 0x3C00u;
    }
    return __builtin_bit_cast(half8, v);
}
DEV half8 pack_v(f32x4 a, f32x4 b) {
    u32x4 v = {pk_f16m(a[0], a[1]), pk_f16m(a[2], a[3]), pk_f16m(b[0], b[1]), pk_f16m(b[2], b[3])};
    return __builtin_bit_cast(half8, v);
}
template <int OFF> DEV void st4rows(unsigned base, unsigned a, unsigned b, unsigned c, unsigned d) {
    asm volatile("s_mov_b32 m0, %4\n\ts_nop 0\n\tds_write_addtid_b32 %0 offset:%5\n\tds_write_addtid_b32 %1 offset:%6\n\t"
                 "ds_write_addtid_b32 %2 offset:%7\n\tds_write_addtid_b32 %3 offset:%8"
                 :: "v"(a), "v"(b), "v"(c), "v"(d), "s"(base), "i"(OFF), "i"(OFF + KG_ROW), "i"(OFF + 2 * KG_ROW), "i"(OFF + 3 * KG_ROW) : "memory");
}
DEV unsigned pkc(cf v) { unsigned r; asm("v_cvt_pk_f16_f32 %0, %1, %2" : "=v"(r) : "v"(v[0]), "v"(v[1])); return r; }
DEV unsigned pkj(cf v) { unsigned r; asm("v_cvt_pk_f16_f32 %0, %1, -%2" : "=v"(r) : "v"(v[0]), "v"(v[1])); return r; }

struct KF { half8 a[4], b[4], c[3], x[4], bx[4]; };

template <int HS>
DEV void conv_kfrags(KF& k, const unsigned char* kb1, const unsigned char* kb2, int fr, int koffb) {
#pragma unroll
    for (int rr = 0; rr < 4; ++rr) {
        const int r = 4 * HS + rr;
        k.a[rr] = kfrag(kb1 + 64 * (16 - r), kb1 + 64 * (16 - r) + 8);
        k.b[rr] = kfrag(kb1 + 64 * (8 - r), kb1 + 64 * (8 - r) + 8);
    }
    constexpr int ea = HS ? 0 : 1, eb = HS ? 3 : 2;
#pragma unroll
    for (int s = 2; s <= 4; ++s)
        k.c[s - 2] = kfrag(kb2 + 2 * (IMG_RC + ea * 64 + 16 * (4 - s)), kb2 + 2 * (IMG_RC + eb * 64 + 16 * (4 - s)));
    if (HS == 0) {
        k.x[0] = kfrag(kb1, kb1 + 8);
        k.x[1] = kfrag(kb1 + 2 * IMG_RX, kb1 + 2 * IMG_RX + 8);
        k.x[2] = k.x[0]; k.x[3] = k.x[0];
    } else {
#pragma unroll
        for (int s = 4; s <= 7; ++s)
            k.x[s - 4] = kfrag(kb2 + 2 * (IMG_RD + 16 * (7 - s)), kb2 + 2 * (IMG_RD + 80 + 16 * (7 - s)));
    }
}

template <int HS, int TG>
DEV void conv_mfma(const unsigned char* Uc, const KF& k, f32x4* red) {
    constexpr int I0 = TG ? 5 : 0, I1 = TG ? 7 : 4, NT = I1 - I0 + 1;
    constexpr int C0 = TG ? 1 : 0, C1 = TG ? 5 : 2;
    constexpr int D1 = TG ? 3 : 0;
    const f32x4 zero4 = {0, 0, 0, 0};
    f32x4 acc[NT];
#pragma unroll
    for (int i = 0; i < NT; ++i) acc[i] = zero4;
    half8 fc[6], fd[4];
#define UFRAG(delta) (*(const half8*)(Uc + 64 * (delta)))
#define ACC(I, a, b) do { if ((I) >= I0 && (I) <= I1) acc[(I) - I0] = MFMA16(a, b, acc[(I) - I0]); } while (0)
    if (HS == 0) {
        constexpr int M0 = TG ? 1 : 0, M1 = I1;
        half8 ub[8];
#pragma unroll
        for (int m = M0; m <= M1; ++m) ub[m] = UFRAG(8 * m);
#pragma unroll
        for (int m = C0; m <= C1; ++m) fc[m] = pack_v(MFMA16(ub[m], k.bx[0], zero4), MFMA16(ub[m], k.bx[1], zero4));
#pragma unroll
        for (int m = M0; m <= M1; ++m) {
            ACC(m, k.a[0], ub[m]); ACC(m + 1, k.b[0], ub[m]); ACC(m + 2, k.x[0], ub[m]); ACC(m + 4, k.x[1], ub[m]);
        }
    } else {
        half8 ua[6], ub[4];
#pragma unroll
        for (int m = 0; m <= C1; ++m) ua[m] = *(const half8*)(Uc + 64 * 8 * m - 16);
#pragma unroll
        for (int m = 0; m <= D1; ++m) ub[m] = UFRAG(8 * m);
#pragma unroll
        for (int m = C0; m <= C1; ++m) fc[m] = pack_v(MFMA16(ua[m], k.bx[0], zero4), MFMA16(ua[m], k.bx[1], zero4));
#pragma unroll
        for (int m = 0; m <= D1; ++m) fd[m] = pack_v(MFMA16(ua[m], k.bx[2], zero4), MFMA16(ub[m], k.bx[3], zero4));
    }
    constexpr int MM0 = TG ? 4 : 0;
#pragma unroll
    for (int rr = (HS == 0 ? 1 : 0); rr < 4; ++rr) {
        const int r = 4 * HS + rr;
        half8 ub[8];
#pragma unroll
        for (int m = MM0; m <= I1; ++m) ub[m] = UFRAG(8 * m - r);
#pragma unroll
        for (int m = MM0; m <= I1; ++m) { ACC(m, k.a[rr], ub[m]); ACC(m + 1, k.b[rr], ub[m]); }
    }
#pragma unroll
    for (int s = 2; s <= 4; ++s)
#pragma unroll
        for (int m = C0; m <= C1; ++m) ACC(m + s, fc[m], k.c[s - 2]);
    if (HS == 1) {
#pragma unroll
        for (int s = 4; s <= I1; ++s)
#pragma unroll
            for (int m = 0; m <= D1; ++m) ACC(m + s, fd[m], k.x[s - 4]);
    }
#undef UFRAG
#undef ACC
#pragma unroll
    for (int i = 0; i < NT; ++i) red[64 * (I0 + i)] = acc[i];
}

DEV void mod_unit_pos(int u, int& cg, int& rg) {
    const int v = u < 147 ? u : u - 147;
    rg = v / 21; cg = v - rg * 21 + (u < 147 ? 0 : 21);
}
DEV void mod_unit_a(u32x4 wv, u16 cval, float* red, int lane, int w) {
    const float cvf = bf2f(cval);
    const float s = bfr(cvf * frcp(1.f + fexp(-cvf)));
    float a[4];
#pragma unroll
    for (int j = 0; j < 4; ++j) a[j] = fold32(s * bflo(wv[j]), s * bfhi(wv[j]));
    float b0 = fold16(a[0], a[1]), b1 = fold16(a[2], a[3]);
    b0 += dpp_f<0x128>(b0); b1 += dpp_f<0x128>(b1);
    if ((lane & 8) == 0) {
        const int col = (lane & 7) * 8 + 2 * ((lane >> 4) & 1) + (lane >> 5);
        red[w * 64 + col] = b0; red[w * 64 + col + 4] = b1;
    }
}
DEV void mod_unit_b(const float* red, int cg, int rg, unsigned tag, rsrc_t rs, int lane) {
    float t = 0.f;
#pragma unroll
    for (int k = 0; k < 8; ++k) t += red[k * 64 + lane];
    st8(rs, WS_MP + (cg * 64 + lane) * 64 + rg * 8, (u32x2){f2u(t), tag});
}

DEV float mp_sum(rsrc_t rs, unsigned off, u32x2 v, int l, unsigned tag) {
#pragma unroll 1
    for (unsigned spins = 0; spins < SPIN_CAP; ++spins) {
        if (wave_all(v[1] == tag || (l & 7) == 7)) break;
        __builtin_amdgcn_s_sleep(2);
        OPAQUE(off);
        v = ld8(rs, off);
    }
    float s = (l & 7) == 7 ? 0.f : u2f(v[0]);
    s += dpp_f<0xB1>(s); s += dpp_f<0x4E>(s); s += dpp_f<0x141>(s);
    return s;
}
DEV void wp_publish(rsrc_t rs, unsigned off, u32x2 v, int l, unsigned tag, int lane, int pair) {
    const float s = mp_sum(rs, off, v, l, tag), s2 = dpp_f<0x128>(s);
    const unsigned pk = umin_(pk_bf16(s, s2), 0xfffffffeu);
    const u32x4 o = {(unsigned)__builtin_amdgcn_readlane((int)pk, 0), (unsigned)__builtin_amdgcn_readlane((int)pk, 16),
                     (unsigned)__builtin_amdgcn_readlane((int)pk, 32), 0u};
    st16(rs, WS_WP + (lane * 224 + pair) * 16, o);
}
#define GAS __attribute__((address_space(1)))
typedef const GAS u16* gptr;
NOPK __global__ __launch_bounds__(512, 2) void fused_block(
    const u16* x_, const u16* A_re_, const u16* A_im_, const u16* B_re_, const u16* B_im_, const u16* C_re_,
    const u16* C_im_, const u16* log_dt_, const u16* Dskip_, const u16* w_r_, const u16* w_gate_, const u16* w_up_,
    const u16* w_down_, const u16* cvec_, const u16* w_mod_, unsigned char* ws, u16* out)
{
    __shared__ __attribute__((aligned(16))) unsigned char smem[SM_TOTAL];
    gptr x = (gptr)x_, A_re = (gptr)A_re_, A_im = (gptr)A_im_, B_re = (gptr)B_re_, B_im = (gptr)B_im_, C_re = (gptr)C_re_;
    gptr C_im = (gptr)C_im_, log_dt = (gptr)log_dt_, Dskip = (gptr)Dskip_, w_r = (gptr)w_r_, w_gate = (gptr)w_gate_;
    gptr w_up = (gptr)w_up_, w_down = (gptr)w_down_, cvec = (gptr)cvec_, w_mod = (gptr)w_mod_;
    asm volatile("" : "+s"(x), "+s"(A_re), "+s"(A_im), "+s"(cvec), "+s"(w_mod), "+s"(w_r)
                 : "s"(B_re), "s"(B_im), "s"(C_re), "s"(C_im), "s"(log_dt), "s"(Dskip), "s"(w_gate), "s"(w_up), "s"(w_down), "s"(out), "s"(ws));
    const int tid = threadIdx.x, lane = tid & 63;
    const int w = __builtin_amdgcn_readfirstlane(tid >> 6);
    const int b = blockIdx.x, xg = b & 7, jj = b >> 3;
    const int g = xg * 32 + jj;
    const bool is_conv = jj < 28;
    const int pair = xg * 28 + jj;
    const int fb = xg * 4 + (jj - 28);
    const int t = 8 * g + w;
    const bool act = lane < 56;
    const int lo = act ? lane : 0;
    const int fr = lane & 15, fq = lane >> 4;
    const rsrc_t rs = __builtin_amdgcn_make_buffer_rsrc(ws, 0, WS_END, 0x00020000);
    const rsrc_t ro = __builtin_amdgcn_make_buffer_rsrc(out, 0, L_ * D_ * 2, 0x00020000);
    float* misc = (float*)(smem + SM_MISC);
    const u32x4 ones = {0xffffffffu, 0xffffffffu, 0xffffffffu, 0xffffffffu};
    STAMP_DECL;
    STAMP(0);

    const u32x4 xv = *(const GAS u32x4*)(x + (size_t)t * D_ + lo * 8);
    __builtin_amdgcn_sched_barrier(0);
    unsigned cwarm;
    {
        unsigned long long pc; unsigned len;
        asm volatile("s_getpc_b64 %0\n.Lcode_mark_a:\n\ts_mov_b32 %1, .Lcode_mark_b-.Lcode_mark_a" : "=s"(pc), "=s"(len));
        unsigned o = (unsigned)(jj * 16 + (lane & 15)) * 128u;
        o = o < len ? o : 0u;
        cwarm = *(const GAS unsigned*)(pc + o);
    }
    const unsigned tag = *(const unsigned*)(ws + WS_E) + 1u;
    cfence();
    int ucg, urg, cg2, rg2, cg3, rg3;
    mod_unit_pos(b, ucg, urg);
    mod_unit_pos(is_conv ? 0 : 256 + fb, cg2, rg2);
    mod_unit_pos(is_conv ? 0 : (fb < 6 ? 288 + fb : 256 + fb), cg3, rg3);
    const unsigned uoff = (unsigned)((tid >> 3) * (6 * D_) + (tid & 7) * 8);
    if (tid < 504) st16(rs, tid < 448 ? WS_W + g * 7168 + tid * 16 : WS_WP + g * 896 + (tid - 448) * 16, ones);
    cfence();
    u32x4 umv = *(const GAS u32x4*)(w_mod + (urg * 64 * (6 * D_) + ucg * 64) + uoff);
    unsigned ucv = cvec[urg * 64 + (tid >> 3)];
    cfence();
    const unsigned pi = is_conv ? (unsigned)(pair * 512 + tid) : (unsigned)(tid >> 3);
    unsigned p_ldt = log_dt[(is_conv ? 2 * pair : 0) + (tid >> 8)];
    unsigned p_are = (is_conv ? A_re : cvec + rg2 * 64)[pi];
    unsigned p_aim = (is_conv ? A_im : cvec + rg3 * 64)[pi];
    unsigned p_bre = B_re[pi], p_bim = B_im[pi], p_cre = C_re[pi], p_cim = C_im[pi];
    typedef u32x4 __attribute__((aligned(8))) u32x4a8;
    const uintptr_t um2b = is_conv ? (uintptr_t)g_ktab.v : (uintptr_t)(w_mod + (rg2 * 64 * (6 * D_) + cg2 * 64));
    u32x4 um2 = *(const GAS u32x4a8*)(um2b + 2u * (is_conv ? 4u * (tid & 255) : uoff));
    u32x4 um3 = *(const GAS u32x4*)((is_conv ? w_r : w_mod + (rg3 * 64 * (6 * D_) + cg3 * 64)) + (is_conv ? 0u : uoff));
    u32x4 wrv = *(const GAS u32x4*)(w_r + lo * 8);
    cfence();
    const int cty = (w & 3) ^ (w >> 2), hs = cty & 1, tg = cty >> 1;
    const int koffb = -16 * (fq >> 1) + 8 * (fq & 1);
    KF kf;
    kf.bx[0] = box_frag(fr, koffb - 8 * hs, hs ? 14 : 3, hs ? 5 : 3);
    kf.bx[1] = box_frag(fr, koffb - 8 * hs, hs ? 11 : 6, hs ? 3 : 5);
    kf.bx[2] = box_frag(fr, koffb - 8, 12, 9);
    kf.bx[3] = box_frag(fr, koffb, 5, 7);
#pragma unroll
    for (int i = 0; i < 4; ++i) PIN(kf.bx[i]);
    cfence();
    float xf32[8];
    {
#pragma unroll
        for (int j = 0; j < 4; ++j) { xf32[2 * j] = bflo(xv[j]); xf32[2 * j + 1] = bfhi(xv[j]); }
        float mb, rinv;
        ln_stats(xf32, act, mb, rinv);
        u32x4 o;
#pragma unroll
        for (int j = 0; j < 4; ++j) {
            const unsigned dd = pk_bf16(xf32[2 * j] - mb, xf32[2 * j + 1] - mb);
            o[j] = pk_bf16(bflo(dd) * rinv, bfhi(dd) * rinv);
        }
        if (act) *(u32x4*)(smem + SM_TILE + (w * 228 + lane * 4) * 4) = o;
    }
    STAMP(1);
    float* mred = (float*)(smem + SM_MR);
    PIN(umv); PIN(ucv);
    mod_unit_a(umv, (u16)ucv, mred, lane, w);
    PIN(cwarm);
    if (!is_conv) {
        PIN(um2); PIN(um3); PIN(p_are); PIN(p_aim);
        mod_unit_a(um2, (u16)p_are, mred + 512, lane, w);
        mod_unit_a(um3, (u16)p_aim, mred + 1024, lane, w);
    }
    bar();
    if (w < 7) {
        const unsigned* tile = (const unsigned*)(smem + SM_TILE);
#pragma unroll
        for (int it = 0; it < 2; ++it) {
            const int e = tid + 448 * it, pr = e >> 2, k = e & 3;
            const u32x4 v = {tile[(2 * k) * 228 + pr], tag, tile[(2 * k + 1) * 228 + pr], tag};
            st16(rs, WS_XG + pr * 16384 + (8 * g + 2 * k) * 8, v);
        }
    } else {
        mod_unit_b(mred, ucg, urg, tag, rs, lane);
        if (!is_conv) {
            mod_unit_b(mred + 512, cg2, rg2, tag, rs, lane);
            mod_unit_b(mred + 1024, cg3, rg3, tag, rs, lane);
        }
    }
    STAMP(2);
    cfence();
    PIN(p_ldt); PIN(p_are); PIN(p_aim); PIN(p_bre); PIN(p_bim); PIN(p_cre); PIN(p_cim);

    u32x4 fgu[14], fdn[8];
    if (is_conv) {
        const int c = w >> 2;
        const unsigned dsk2 = *(const GAS unsigned*)(Dskip + 2 * pair);
        const int mpl = lane < 48 ? lane : 0;
        const unsigned mpoff = WS_MP + ((mpl >> 4) * D_ + 2 * pair) * 64 + (mpl & 15) * 8;
        u32x2 mpv = {0, 0};
        {
            const float dt = bfr(expf(bflo(p_ldt)));
            float zr = dt * bflo(p_are);
            const float zi = dt * bflo(p_aim);
            const float dbr = dt * bflo(p_bre), dbi = dt * bflo(p_bim);
            const float cre = bflo(p_cre), cim = bflo(p_cim);
            float cbr = cre * dbr - cim * dbi, cbi = cre * dbi + cim * dbr;
            asm("" : "+v"(cbr), "+v"(cbi));
            float mx, rho;
            {
                const unsigned a = f2u(__builtin_fmaxf(__builtin_fabsf(cbr), __builtin_fabsf(cbi)));
                const unsigned bb = f2u(__builtin_fmaxf(zr, 0.f));
                auto r = __builtin_amdgcn_permlane32_swap(a, bb, false, false);
                unsigned v = umax_(r[0], r[1]);
                { auto q = __builtin_amdgcn_permlane16_swap(v, v, false, false); v = umax_(q[0], q[1]); }
                v = umax_(v, dpp_u<0xB1>(v)); v = umax_(v, dpp_u<0x4E>(v));
                v = umax_(v, dpp_u<0x141>(v)); v = umax_(v, dpp_u<0x140>(v));
                mx = rdlane(u2f(v), 0); rho = rdlane(u2f(v), 32);
            }
            zr -= rho;
            const int ew = exp_above(mx);
            const float sc = pow2i(14 - ew);
            const cf q0 = {cbr * sc, cbi * sc};
            const float t2 = zr * 1.44269504088896341f;
            const float rh = zi * 0.15915494309189535f;
            const float rl = __builtin_fmaf(zi, 0.15915494309189535f, -rh) + zi * 6.4206382e-09f;
            unsigned char* wb = smem + SM_KG + w * KG_WAVE;
            const unsigned wba = (unsigned)(size_t)(LAS unsigned char*)wb;
            const unsigned char* rd0 = wb + fr * KG_ROW + fq * 16;
            const unsigned char* rd1 = rd0 + KG_BUF;
            const cf s1 = cexp_pow2(t2, rh, rl, 16.f), p1 = cexp_pow2(t2, rh, rl, 1.f), g1 = cexp_pow2(t2, rh, rl, 256.f);
            STAMP(3);
            cf s2, p2, g2, s4, p4, g4, s8, p8, g8, q1, q2, q3, q4, q5, q6, q7, p3, p5, p6, p7, g3, u0, u1, u2, u3;
            cmul4(s2, s1, s1, p2, p1, p1, g2, g1, g1, q1, q0, s1);
            cmul4(s4, s2, s2, p4, p2, p2, g4, g2, g2, q2, q0, s2);
            cmul4(s8, s4, s4, p8, p4, p4, q3, q1, s2, p3, p2, p1);
            st4rows<0>(wba, pkj(q0), pkj(q1), pkj(q2), pkj(q3));
            cmul4(q4, q0, s4, q5, q1, s4, q6, q2, s4, q7, q3, s4);
            st4rows<KG_BUF>(wba, 0x00003C00u, pkc(p1), pkc(p2), pkc(p3));
            cmul4(u0, q0, s8, u1, q1, s8, u2, q2, s8, u3, q3, s8);
            st4rows<4 * KG_ROW>(wba, pkj(q4), pkj(q5), pkj(q6), pkj(q7));
            cmul4(p5, p4, p1, p6, p4, p2, p7, p4, p3, g3, g2, g1);
            st4rows<8 * KG_ROW>(wba, pkj(u0), pkj(u1), pkj(u2), pkj(u3));
            cmul4(u0, q4, s8, u1, q5, s8, u2, q6, s8, u3, q7, s8);
            st4rows<KG_BUF + 4 * KG_ROW>(wba, pkc(p4), pkc(p5), pkc(p6), pkc(p7));
            cf p10, p12, p14, v0, v1, v2, v3;
            cmul4(v0, p8, p1, p10, p8, p2, v1, p8, p3, p12, p8, p4);
            st4rows<12 * KG_ROW>(wba, pkj(u0), pkj(u1), pkj(u2), pkj(u3));
            cmul4(v2, p8, p5, p14, p8, p6, v3, p8, p7, g8, g4, g4);
            st4rows<KG_BUF + 8 * KG_ROW>(wba, pkc(p8), pkc(v0), pkc(p10), pkc(v1));
            st4rows<KG_BUF + 12 * KG_ROW>(wba, pkc(p12), pkc(v2), pkc(p14), pkc(v3));
            STAMP(22);
            PIN(um2); PIN(wrv);
            half8 xf[4], ff[4];
            f32x4 acc0 = {0, 0, 0, 0}, acc1 = {0, 0, 0, 0}, acc2 = {0, 0, 0, 0};
#pragma unroll
            for (int s = 0; s < 4; ++s) { xf[s] = *(const half8*)(rd0 + s * 64); ff[s] = *(const half8*)(rd1 + s * 64); }
            cfence();
            STAMP(23);
            {
                cf g5, g6, g7;
                cmul3(u0, g1, p2, u1, g1, p4, u2, g1, p6);
                cmul4(v0, g1, p8, v1, g1, p10, v2, g1, p12, v3, g1, p14);
                st4rows<0>(wba, pkc(g1), pkc(u0), pkc(u1), pkc(u2));
                cmul3(u0, g2, p4, u1, g2, p8, u2, g2, p12);
                st4rows<4 * KG_ROW>(wba, pkc(v0), pkc(v1), pkc(v2), pkc(v3));
                cmul3(v0, g3, p4, v1, g3, p8, v2, g3, p12);
                st4rows<8 * KG_ROW>(wba, pkc(g2), pkc(u0), pkc(u1), pkc(u2));
                cmul4(g5, g4, g1, g6, g4, g2, g7, g4, g3, u0, g4, p8);
                st4rows<12 * KG_ROW>(wba, pkc(g3), pkc(v0), pkc(v1), pkc(v2));
                cmul3(u1, g5, p8, u2, g6, p8, u3, g7, p8);
                *(unsigned*)(wb + KG_BUF + 8 * KG_ROW + lane * 4) = pkc(g8);
                st4rows<KG_BUF>(wba, pkc(g4), pkc(u0), pkc(g5), pkc(u1));
                st4rows<KG_BUF + 4 * KG_ROW>(wba, pkc(g6), pkc(u2), pkc(g7), pkc(u3));
            }
#pragma unroll
            for (int s = 0; s < 4; ++s) acc0 = MFMA16(xf[s], ff[s], acc0);
            cfence();
#pragma unroll
            for (int s = 0; s < 4; ++s) {
                acc1 = MFMA16(xf[s], *(const half8*)(rd0 + s * 64), acc1);
                acc2 = MFMA16(xf[s], *(const half8*)(rd1 + s * 64), acc2);
            }
            cfence();
            if (w == 7) mpv = ld8(rs, mpoff);
            const float us = pow2i(ew - 14);
            acc0 *= us; acc1 *= us; acc2 *= us;
            if (rho > 0.f) {
                const float rw = rho * 1.44269504088896341f;
                const int h1 = fr < 8 ? 256 + 2 * fr : 512 + 256 * ((fr - 8) >> 2) + 4 * (fr & 3);
                const int h2 = fr < 8 ? 1024 + 256 * (fr >> 1) + 8 * (fr & 1) : 2048;
#pragma unroll 1
                for (int j = 0; j < 4; ++j) {
                    const int lo16 = 16 * (4 * fq + j);
                    const float e0 = exp2f(rw * (float)(fr + lo16)), e1 = exp2f(rw * (float)(h1 + lo16));
                    const float e2 = exp2f(rw * (float)(h2 + lo16 > 2048 ? 2048 : h2 + lo16));
#pragma unroll
                    for (int q = 0; q < 4; ++q)
                        if (q == j) { acc0[q] *= e0; acc1[q] *= e1; acc2[q] *= e2; }
                }
            }
            float* P = (float*)wb;
            *(f32x4*)(P + (0 + fr) * 20 + 4 * fq) = acc0;
            *(f32x4*)(P + (16 + fr) * 20 + 4 * fq) = acc1;
            *(f32x4*)(P + (32 + fr) * 20 + 4 * fq) = acc2;
            const float eb = (float)ew + __builtin_fminf(__builtin_ceilf(rho * (2048.f * 1.44269504088896341f)), 100.f);
            if (lane == 0) misc[w] = eb;
        }
        STAMP(4);
        bar();
        int eM0, eM1;
        {
            const f32x4 m0 = *(const f32x4*)misc, m1 = *(const f32x4*)(misc + 4);
            eM0 = (int)__builtin_fminf(__builtin_fmaxf(__builtin_fmaxf(m0[0], m0[1]), __builtin_fmaxf(m0[2], m0[3])), 110.f);
            eM1 = (int)__builtin_fminf(__builtin_fmaxf(__builtin_fmaxf(m1[0], m1[1]), __builtin_fmaxf(m1[2], m1[3])), 110.f);
        }
        {
            const int th = tid & 255;
            unsigned char* T16 = smem + SM_T16 + c * 1600;
            if (th < 192) {
                const float* P = (const float*)(smem + SM_KG + c * 4 * KG_WAVE) + (th >> 2) * 20 + 4 * (th & 3);
                f32x4 s = *(const f32x4*)P + *(const f32x4*)(P + KG_WAVE / 4);
                s += *(const f32x4*)(P + 2 * (KG_WAVE / 4)) + *(const f32x4*)(P + 3 * (KG_WAVE / 4));
                s *= pow2i(6 - (c ? eM1 : eM0));
                *(u32x2*)(T16 + th * 8) = (u32x2){pk_f16(s[0], s[1]), pk_f16(s[2], s[3])};
            } else {
                if (th == 192) *(unsigned*)(T16 + 1536) = 0;
                *(u32x2*)(smem + SM_U + c * (U_LEN * 2) + lane * 8) = (u32x2){0, 0};
                if (w == 7) {
                    const float s = mp_sum(rs, mpoff, mpv, mpl, tag);
                    if (lane < 48 && (lane & 7) == 0) misc[16 + (lane >> 3)] = bfr(s);
                    mpv = ld8(rs, mpoff + 3 * D_ * 64);
                }
            }
        }
        bar();
        unsigned xoff = WS_XG + pair * 16384 + tid * 32;
        u32x4 g0 = ld16(rs, xoff), g1 = ld16(rs, xoff + 16);
        cfence();
        {
            const u32x2 kix0 = {um2[0], um2[1]}, kix1 = {um2[2], um2[3]};
            const unsigned char* T16 = smem + SM_T16 + c * 1600;
            const unsigned e0 = *(const u16*)(T16 + (kix0[0] & 0xffff)), e1 = *(const u16*)(T16 + (kix0[0] >> 16));
            const unsigned e2 = *(const u16*)(T16 + (kix0[1] & 0xffff)), e3 = *(const u16*)(T16 + (kix0[1] >> 16));
            const unsigned e4 = *(const u16*)(T16 + (kix1[0] & 0xffff)), e5 = *(const u16*)(T16 + (kix1[0] >> 16));
            const unsigned e6 = *(const u16*)(T16 + (kix1[1] & 0xffff));
            const unsigned p01 = e0 | (e1 << 16), p12 = e1 | (e2 << 16), p23 = e2 | (e3 << 16);
            const unsigned p34 = e3 | (e4 << 16), p45 = e4 | (e5 << 16), p56 = e5 | (e6 << 16);
            unsigned char* kc = smem + SM_KC + c * 4 * KC_STR + (tid & 255) * 8;
            *(u32x2*)(kc) = (u32x2){p01, p23};
            *(u32x2*)(kc + KC_STR) = (u32x2){p12, p34};
            *(u32x2*)(kc + 2 * KC_STR) = (u32x2){p23, p45};
            *(u32x2*)(kc + 3 * KC_STR) = (u32x2){p34, p56};
        }
        bar();
        STAMP(5);
        {
            const unsigned char* kcb = smem + SM_KC + c * 4 * KC_STR + ((3 - fr) & 3) * KC_STR;
            const unsigned char* kb1 = kcb + ((31 + koffb - fr) & ~3) * 2;
            const unsigned char* kb2 = kcb + ((15 - fr + 4 * fq) & ~3) * 2;
            if (hs == 0) conv_kfrags<0>(kf, kb1, kb2, fr, koffb); else conv_kfrags<1>(kf, kb1, kb2, fr, koffb);
#pragma unroll
            for (int i = 0; i < 4; ++i) { PIN(kf.a[i]); PIN(kf.b[i]); PIN(kf.x[i]); }
#pragma unroll
            for (int i = 0; i < 3; ++i) PIN(kf.c[i]);
        }
        const f32x4 mA = *(const f32x4*)(misc + 16);
        const float ga0 = misc[20], ga1 = misc[21];
        const float opc0 = bfr(1.f + mA[2]), opc1 = bfr(1.f + mA[3]);
        const int eu0 = exp_above(32.f * __builtin_fabsf(opc0) + __builtin_fabsf(mA[0]));
        const int eu1 = exp_above(32.f * __builtin_fabsf(opc1) + __builtin_fabsf(mA[1]));
        const float su0 = pow2i(11 - eu0), su1 = pow2i(11 - eu1), iu0 = pow2i(eu0 - 11), iu1 = pow2i(eu1 - 11);
        float oc0 = opc0 * su0, oc1 = opc1 * su1, sa0 = mA[0] * su0, sa1 = mA[1] * su1;
        float dk0 = bflo(dsk2) * iu0, dk1 = bfhi(dsk2) * iu1;
        float ys0 = pow2i(eM0 - 6) * iu0, ys1 = pow2i(eM1 - 6) * iu1;
        PIN(oc0); PIN(oc1); PIN(sa0); PIN(sa1); PIN(dk0); PIN(dk1); PIN(ys0); PIN(ys1);
        STAMP(6);
#pragma unroll 1
        for (unsigned spins = 0; spins < SPIN_CAP; ++spins) {
            if (wave_all(((g0[1] ^ tag) | (g0[3] ^ tag) | (g1[1] ^ tag) | (g1[3] ^ tag)) == 0)) break;
            OPAQUE(xoff);
            g0 = ld16(rs, xoff);
            g1 = ld16(rs, xoff + 16);
        }
        STAMP(7);
        unsigned hd[4];
        {
            const unsigned xn[4] = {g0[0], g0[2], g1[0], g1[2]};
            float h0[4], h1[4];
#pragma unroll
            for (int j = 0; j < 4; ++j) {
                const unsigned a = pk_bf16(bflo(xn[j]) * oc0, bfhi(xn[j]) * oc1);
                const unsigned h = pk_bf16(bflo(a) + sa0, bfhi(a) + sa1);
                h0[j] = bflo(h); h1[j] = bfhi(h);
            }
            unsigned char* U0 = smem + SM_U + (U_PAD + 4 * tid) * 2;
            *(u32x2*)(U0) = (u32x2){pk_f16(h0[0], h0[1]), pk_f16(h0[2], h0[3])};
            *(u32x2*)(U0 + U_LEN * 2) = (u32x2){pk_f16(h1[0], h1[1]), pk_f16(h1[2], h1[3])};
            bar();
#pragma unroll
            for (int j = 0; j < 4; ++j) hd[j] = pk_bf16(h0[j] * dk0, h1[j] * dk1);
        }
        STAMP(8);
        if (b == 0 && tid == 0) st4(rs, WS_E, tag);
        if (w == 7) wp_publish(rs, mpoff + 3 * D_ * 64, mpv, mpl, tag, lane, pair);
        st16(rs, WS_H2 + pair * 8192 + tid * 16, ones);
        if (tid < 128) st16(rs, WS_PART + pair * 2048 + tid * 16, ones);
        else if (tid < 130 && pair < 129) st16(rs, WS_R + pair * 32 + (tid - 128) * 16, (u32x4){0, 0, 0, 0});
        cfence();
        unsigned warm;
        {
            const int it = jj * 120 + (tid < 120 ? tid : 0);
            gptr p = it < 1792 ? w_up + ((it >> 2) * F_ + xg * 224 + (it & 3) * 64) : w_down + (xg * 224 * D_ + (it - 1792) * 64);
            warm = *(const GAS unsigned*)p;
        }
        cfence();
        {
            const unsigned char* Uc = smem + SM_U + c * (U_LEN * 2) + (U_PAD + 16 * fr + koffb) * 2;
            f32x4* red = (f32x4*)(smem + SM_KG) + (c * 2 + hs) * 512 + 4 * fr + (fq ^ ((fr >> 1) & 3));
            if (hs == 0) { if (tg == 0) conv_mfma<0, 0>(Uc, kf, red); else conv_mfma<0, 1>(Uc, kf, red); }
            else         { if (tg == 0) conv_mfma<1, 0>(Uc, kf, red); else conv_mfma<1, 1>(Uc, kf, red); }
        }
        STAMP(9);
        asm volatile("s_waitcnt vmcnt(1)" ::: "memory");
        bar();
        {
            const f32x4* red = (const f32x4*)(smem + SM_KG) + ((tid & ~3) | ((tid ^ (tid >> 3)) & 3));
            const f32x4 y0 = (red[0] + red[512]) * ys0;
            const f32x4 y1 = (red[1024] + red[1536]) * ys1;
            u32x4 o;
#pragma unroll
            for (int j = 0; j < 4; ++j) {
                const unsigned y = pk_bf16(y0[j], y1[j]);
                const unsigned z = pk_bf16(bflo(y) + bflo(hd[j]), bfhi(y) + bfhi(hd[j]));
                o[j] = umin_(pk_bf16(ga0 * bflo(z), ga1 * bfhi(z)), 0xfffffffeu);
            }
            st16(rs, WS_W + ((tid >> 3) * 224 + pair) * 128 + (tid & 7) * 16, o);
        }
        PIN(warm);
        STAMP(10);
    } else {
        STAMP(4);
#pragma unroll 1
        for (int base = w * 64; base < 6272; base += 512) {
            const int ch = base + lane, mat = ch >= 3136, cc = ch - mat * 3136, row = cc / 7, kk = cc - row * 7;
            __builtin_amdgcn_global_load_lds((const GAS unsigned*)((mat ? w_up : w_gate) + (size_t)row * F_ + fb * 56 + kk * 8),
                                             (LAS unsigned*)(smem + SM_KG + base * 16), 16, 0, 0);
        }
        bar();
#pragma unroll 1
        for (int base = w * 64; base < 3136; base += 512)
            __builtin_amdgcn_global_load_lds((const GAS unsigned*)(w_down + (size_t)fb * 56 * D_ + (base + lane) * 8),
                                             (LAS unsigned*)(smem + SM_KG + 100352 + base * 16), 16, 0, 0);
        if (tid < 16) *(u32x4*)(smem + SM_SEL + tid * 16) = (u32x4){0, 0, 0, 0};
        STAMP(5);
        {
            unsigned off = WS_XG + (unsigned)(8 * ((g & ~3) + (lane & 3))) * 8;
#pragma unroll 1
            for (unsigned spins = 0; spins < SPIN_CAP; ++spins) {
                OPAQUE(off);
                if (wave_all(ld8(rs, off)[1] == tag)) break;
            }
        }
        STAMP(10);
    }

    STAMP(11);
    if (w < 7) {
        u32x4 v, pv;
        unsigned off = WS_W + ((g >> 2) * 224 + (tid >> 1)) * 128 + (g & 3) * 32 + (tid & 1) * 16;
        const unsigned poff = WS_WP + ((g >> 2) * 224 + (tid >> 1)) * 16;
#pragma unroll 1
        for (unsigned spins = 0; spins < SPIN_CAP; ++spins) {
            OPAQUE(off);
            v = ld16(rs, off);
            pv = ld16(rs, poff);
            if (wave_all(valid4(v) && valid4(pv))) break;
            if (!is_conv) __builtin_amdgcn_s_sleep(6);
        }
        unsigned* tile = (unsigned*)(smem + SM_TILE) + (tid & 1) * 4 * 228 + (tid >> 1);
#pragma unroll
        for (int j = 0; j < 4; ++j) tile[j * 228] = v[j];
        f32x2* prm = (f32x2*)(smem + SM_PRM) + (tid >> 1);
        if (tid & 1) {
            const unsigned a = pk_bf16(1.f + bflo(pv[1]), 1.f + bfhi(pv[1]));
            prm[224] = (f32x2){bflo(a), bfhi(a)};
        } else {
            prm[0] = (f32x2){bflo(pv[0]), bfhi(pv[0])};
            prm[448] = (f32x2){bflo(pv[2]), bfhi(pv[2])};
        }
    }
    STAMP(12);
    bar();
    u32x4 orow;
    {
        const u32x4 wv = *(const u32x4*)(smem + SM_TILE + (w * 228 + lo * 4) * 4);
        float sbf[8], ocf[8], gbf[8];
        {
            const f32x4* pp = (const f32x4*)(smem + SM_PRM) + lo * 2;
#pragma unroll
            for (int h = 0; h < 2; ++h) {
                const f32x4 a = pp[h], c2 = pp[112 + h], d2 = pp[224 + h];
#pragma unroll
                for (int j = 0; j < 4; ++j) { sbf[4 * h + j] = a[j]; ocf[4 * h + j] = c2[j]; gbf[4 * h + j] = d2[j]; }
            }
        }
        float x1[8];
#pragma unroll
        for (int j = 0; j < 4; ++j) {
            const unsigned a = pk_bf16(xf32[2 * j] + bflo(wv[j]), xf32[2 * j + 1] + bfhi(wv[j]));
            x1[2 * j] = bflo(a); x1[2 * j + 1] = bfhi(a);
        }
        float mb, rinv;
        ln_stats(x1, act, mb, rinv);
        u32x4 hp;
        float r = 0.f;
#pragma unroll
        for (int j = 0; j < 4; ++j) {
            const unsigned dd = pk_bf16(x1[2 * j] - mb, x1[2 * j + 1] - mb);
            const unsigned xn = pk_bf16(bflo(dd) * rinv, bfhi(dd) * rinv);
            const unsigned a = pk_bf16(bflo(xn) * ocf[2 * j], bfhi(xn) * ocf[2 * j + 1]);
            hp[j] = pk_bf16(bflo(a) + sbf[2 * j], bfhi(a) + sbf[2 * j + 1]);
            r = dot2bf(hp[j], wrv[j], r);
        }
        r = wave_sum(act ? r : 0.f);
        const unsigned rb = pk_bf16(r, r) & 0xffffu;
        const unsigned mono = (rb & 0x8000u) ? (~rb & 0xffffu) : (rb | 0x8000u);
        if (lane == 0) *(u16*)(smem + SM_MISC + 128 + w * 2) = (u16)umax_(mono, 1u);
        bar();
        if (tid == 0) st16(rs, WS_R + ((g & 7) * 32 + (g >> 3)) * 16, *(const u32x4*)(smem + SM_MISC + 128));
        STAMP(13);
        cfence();
        {
            u32x4 hq;
#pragma unroll
            for (int j = 0; j < 4; ++j) hq[j] = umin_(hp[j], 0xfffffffeu);
            if (act) st16(rs, WS_H2 + t * 896 + lane * 16, hq);
        }
        cfence();
#pragma unroll
        for (int j = 0; j < 4; ++j) {
            const unsigned a = pk_bf16(gbf[2 * j] * bflo(hp[j]), gbf[2 * j + 1] * bfhi(hp[j]));
            orow[j] = pk_bf16(x1[2 * j] + bflo(a), x1[2 * j + 1] + bfhi(a));
        }
        if (is_conv) {
            unsigned* lsel = (unsigned*)(smem + SM_SEL);
            if (w == 0) {
                unsigned off = WS_SEL + (lane & 7) * 4, sv;
#pragma unroll 1
                for (unsigned spins = 0; spins < SPIN_CAP; ++spins) {
                    __builtin_amdgcn_s_sleep(6);
                    OPAQUE(off);
                    sv = ld4(rs, off);
                    if (wave_all(sv != 0u)) break;
                }
                if (lane < 8) lsel[lane] = sv;
            }
            bar();
            const unsigned sk = lsel[lane & 7];
            if (wave_all((sk & 0xffffu) != (unsigned)(2047 - t)) && act) *(GAS u32x4*)(out + (size_t)t * D_ + lane * 8) = orow;
            STAMP(14);
            STAMP_FLUSH;
            return;
        }
    }

    unsigned* slots = (unsigned*)(smem + SM_SEL);
    unsigned* wmax = slots + 64;
    unsigned* sel = slots + 72;
    unsigned char* atile = smem + SM_PRM;
    unsigned k4[4];
    {
        const int wq = w < 7 ? w : 0;
        const unsigned char* p = smem + SM_KG + ((fr >> 1) & 1) * 50176 + (8 * fq + (fr >> 2)) * 112 + (8 * wq + 4 * (fr & 1)) * 2;
#pragma unroll
        for (int ks = 0; ks < 14; ++ks) {
            const u32x2 a = tr_read(p + ks * 32 * 112), c2 = tr_read(p + ks * 32 * 112 + 4 * 112);
            fgu[ks] = (u32x4){a[0], a[1], c2[0], c2[1]};
        }
#pragma unroll
        for (int ks = 0; ks < 2; ++ks)
#pragma unroll
            for (int i = 0; i < 4; ++i) {
                int r0 = 32 * ks + 8 * fq + (fr >> 2), r1 = r0 + 4;
                r0 = r0 > 55 ? 55 : r0; r1 = r1 > 55 ? 55 : r1;
                const unsigned char* q = smem + SM_KG + 100352 + (16 * (4 * wq + i) + 4 * (fr & 3)) * 2;
                const u32x2 a = tr_read(q + r0 * 896), c2 = tr_read(q + r1 * 896);
                fdn[ks * 4 + i] = (u32x4){a[0], a[1], c2[0], c2[1]};
            }
#pragma unroll
        for (int ks = 0; ks < 14; ++ks) PIN(fgu[ks]);
#pragma unroll
        for (int ks = 0; ks < 8; ++ks) PIN(fdn[ks]);
        if (tid < 8) *(u32x4*)(atile + tid * 144 + 112) = (u32x4){0, 0, 0, 0};
    }
    {
        u32x2 rv;
        unsigned off = WS_R + w * 512 + lane * 8;
#pragma unroll 1
        for (unsigned spins = 0; spins < SPIN_CAP; ++spins) {
            OPAQUE(off);
            rv = ld8(rs, off);
            if (wave_all(rv[0] != 0u && rv[1] != 0u)) break;
        }
        const unsigned tb = 2047u - (unsigned)(64 * (lane >> 1) + 8 * w + 4 * (lane & 1));
        k4[0] = (rv[0] << 16) | tb; k4[1] = (rv[0] & 0xffff0000u) | (tb - 1u);
        k4[2] = (rv[1] << 16) | (tb - 2u); k4[3] = (rv[1] & 0xffff0000u) | (tb - 3u);
    }
    STAMP(15);
    {
        const unsigned m = wave_umax(umax_(umax_(k4[0], k4[1]), umax_(k4[2], k4[3])));
        if (lane == 0) wmax[w] = m;
    }
    bar();
    {
        const u32x4 a = *(const u32x4*)wmax, c2 = *(const u32x4*)(wmax + 4);
        const unsigned tau = umin_(umin_(umin_(a[0], a[1]), umin_(a[2], a[3])), umin_(umin_(c2[0], c2[1]), umin_(c2[2], c2[3])));
        unsigned long long mk[4];
        int cnt = 0;
#pragma unroll
        for (int j = 0; j < 4; ++j) { mk[j] = __builtin_amdgcn_ballot_w64(k4[j] >= tau); cnt += __builtin_popcountll(mk[j]); }
        if (cnt <= 8) {
            int base = 0;
#pragma unroll
            for (int j = 0; j < 4; ++j) {
                const int pos = base + (int)__builtin_amdgcn_mbcnt_hi((unsigned)(mk[j] >> 32), __builtin_amdgcn_mbcnt_lo((unsigned)mk[j], 0u));
                if (k4[j] >= tau) slots[w * 8 + pos] = k4[j];
                base += __builtin_popcountll(mk[j]);
            }
        } else {
#pragma unroll 1
            for (int it = 0; it < 8; ++it) {
                const unsigned m = wave_umax(umax_(umax_(k4[0], k4[1]), umax_(k4[2], k4[3])));
#pragma unroll
                for (int j = 0; j < 4; ++j) k4[j] = k4[j] == m ? 0u : k4[j];
                if (lane == 0) slots[w * 8 + it] = m;
            }
        }
    }
    bar();
    unsigned char* h2t = smem + SM_TILE;
    {
        const unsigned key = slots[tid >> 3];
        const u32x4 a = *(const u32x4*)(slots + 8 * (tid & 7)), c2 = *(const u32x4*)(slots + 8 * (tid & 7) + 4);
        unsigned rank = (a[0] > key) + (a[1] > key) + (a[2] > key) + (a[3] > key) + (c2[0] > key) + (c2[1] > key) + (c2[2] > key) + (c2[3] > key);
        rank += dpp_u<0xB1>(rank); rank += dpp_u<0x4E>(rank); rank += dpp_u<0x141>(rank);
        const bool mine = (tid & 7) == 0 && key != 0u && rank < 8u;
        if (mine) sel[rank] = key;
    }
    bar();
    STAMP(16);
    if (fb == 31 && tid < 2) st16(rs, WS_SEL + tid * 16, *(const u32x4*)(sel + 4 * tid));
    const unsigned keyW = sel[w];
    const int tW = 2047 - (int)(keyW & 0xffffu);
    {
        u32x4 v;
        unsigned off = WS_H2 + tW * 896 + lo * 16;
#pragma unroll 1
        for (unsigned spins = 0; spins < SPIN_CAP; ++spins) {
            OPAQUE(off);
            v = ld16(rs, off);
            if (wave_all(valid4(v))) break;
        }
        if (act) *(u32x4*)(h2t + w * 912 + lane * 16) = v;
    }
    STAMP(17);
    bar();
    const int cb4 = 16 * (fb < 28 ? fb : 0) + 4 * (lane & 3);
    const u32x2 fx = *(const GAS u32x2*)(x + (size_t)tW * D_ + cb4);
    const unsigned fw0 = ld4(rs, WS_W + ((tW >> 5) * 224 + (cb4 >> 1)) * 128 + (tW & 31) * 4);
    const unsigned fw1 = ld4(rs, WS_W + ((tW >> 5) * 224 + (cb4 >> 1) + 1) * 128 + (tW & 31) * 4);
    if (w < 7) {
        f32x4 acc = {0, 0, 0, 0}, acb = {0, 0, 0, 0};
        const unsigned char* ap = h2t + (lane & 7) * 912 + fq * 16;
#pragma unroll
        for (int ks = 0; ks < 14; ks += 2) {
            acc = MFMAB(*(const bf16x8*)(ap + ks * 64), __builtin_bit_cast(bf16x8, fgu[ks]), acc);
            acb = MFMAB(*(const bf16x8*)(ap + ks * 64 + 64), __builtin_bit_cast(bf16x8, fgu[ks + 1]), acb);
        }
        acc += acb;
        const int idx = 2 * (fr >> 3) + (fq >> 1);
        const float lo2 = (idx & 1) ? acc[1] : acc[0], hi2 = (idx & 1) ? acc[3] : acc[2];
        const float keep = (idx & 2) ? hi2 : lo2, send = (idx & 2) ? lo2 : hi2;
        const float recv = dpp_f<0x128>(send);
        const unsigned gu = pk_bf16(fr < 8 ? keep : recv, fr < 8 ? recv : keep);
        const float gg = bflo(gu), uu = bfhi(gu);
        const float sg = bfr(gg * frcp(1.f + fexp(-gg)));
        *(u16*)(atile + (4 * (fq & 1) + idx) * 144 + (8 * w + (fr & 7)) * 2) = (u16)pk_bf16(sg * uu, 0.f);
    }
    bar();
    STAMP(18);
    if (w < 7) {
        const bf16x8 a0 = *(const bf16x8*)(atile + (lane & 7) * 144 + fq * 16);
        const bf16x8 a1 = *(const bf16x8*)(atile + (lane & 7) * 144 + 64 + fq * 16);
        f32x4 acc[4];
#pragma unroll
        for (int i = 0; i < 4; ++i) acc[i] = MFMAB(__builtin_bit_cast(bf16x8, fdn[i]), a0, ((f32x4){0, 0, 0, 0}));
#pragma unroll
        for (int i = 0; i < 4; ++i) acc[i] = MFMAB(__builtin_bit_cast(bf16x8, fdn[4 + i]), a1, acc[i]);
#pragma unroll
        for (int i = 0; i < 2; ++i) {
            u32x4 o;
#pragma unroll
            for (int j = 0; j < 4; ++j) o[j] = umin_(f2u(fr < 8 ? acc[i][j] : acc[2 + i][j]), 0xfffffffeu);
            st16(rs, WS_PART + ((4 * w + i + ((fr >> 2) & 2)) * 32 + fb) * 512 + (fr & 7) * 64 + fq * 16, o);
        }
    }
    STAMP(19);
    {
        const unsigned sk = sel[lane & 7];
        if (wave_all((sk & 0xffffu) != (unsigned)(2047 - t)) && act) *(GAS u32x4*)(out + (size_t)t * D_ + lane * 8) = orow;
    }
    if (fb < 28) {
        const unsigned mono = keyW >> 16;
        const unsigned rb = (mono & 0x8000u) ? (mono & 0x7fffu) : (~mono & 0xffffu);
        float rsel = bfr(frcp(1.f + fexp(-u2f(rb << 16))));
        unsigned x1a = pk_bf16(bflo(fx[0]) + bflo(fw0), bfhi(fx[0]) + bfhi(fw0));
        unsigned x1b = pk_bf16(bflo(fx[1]) + bflo(fw1), bfhi(fx[1]) + bfhi(fw1));
        u32x2 h2v = *(const u32x2*)(h2t + w * 912 + cb4 * 2);
        f32x4 gbv = *(const f32x4*)(smem + SM_PRM + (896 + cb4) * 4);
        PIN(rsel); PIN(x1a); PIN(x1b); PIN(h2v); PIN(gbv);
        u32x4 c0, c1;
        unsigned off = WS_PART + (fb * 32 + (lane >> 2)) * 512 + w * 64 + (lane & 3) * 16;
#pragma unroll 1
        for (unsigned spins = 0; spins < SPIN_CAP; ++spins) {
            OPAQUE(off);
            c0 = ld16(rs, off);
            c1 = ld16(rs, off + 16 * 512);
            if (wave_all(valid4(c0) && valid4(c1))) break;
        }
        STAMP(20);
        float v[4];
#pragma unroll
        for (int j = 0; j < 4; ++j) v[j] = u2f(c0[j]) + u2f(c1[j]);
        float u = fold16(fold32(v[0], v[2]), fold32(v[1], v[3]));
        u += dpp_f<0x128>(u);
        u += dpp_f<0x124>(u);
        const int j = fq;
        const unsigned xw = (j & 2) ? x1b : x1a, hw = (j & 2) ? h2v[1] : h2v[0];
        const float x1 = (j & 1) ? bfhi(xw) : bflo(xw), h2 = (j & 1) ? bfhi(hw) : bflo(hw);
        const float gb = (j & 2) ? ((j & 1) ? gbv[3] : gbv[2]) : ((j & 1) ? gbv[1] : gbv[0]);
        const float y2 = bfr(bfr(u) * rsel);
        const float bb = bfr(h2 + y2);
        const float o = x1 + bfr(gb * bb);
        if ((lane & 12) == 0) st2(ro, tW * 896 + (cb4 + j) * 2, pk_bf16(o, o) & 0xffffu);
        STAMP(21);
    }
    asm volatile(".Lcode_mark_b:");
    STAMP_FLUSH;
}

extern "C" void kernel_launch(void* const* d_in, const int* in_sizes, int n_in,
                              void* d_out, int out_size, void* d_ws, size_t ws_size,
                              hipStream_t stream) {
    fused_block<<<256, 512, 0, stream>>>(
        (const u16*)d_in[0], (const u16*)d_in[1], (const u16*)d_in[2], (const u16*)d_in[3], (const u16*)d_in[4],
        (const u16*)d_in[5], (const u16*)d_in[6], (const u16*)d_in[7], (const u16*)d_in[8], (const u16*)d_in[10],
        (const u16*)d_in[11], (const u16*)d_in[12], (const u16*)d_in[13], (const u16*)d_in[14], (const u16*)d_in[15],
        (unsigned char*)d_ws, (u16*)d_out);
}
```
